```python
import torch

def _softmax(x, axis=-1):
    return torch.softmax(x, dim=axis)
def _gelu(x):
    return torch.nn.functional.gelu(x, approximate="tanh")
def _silu(x):
    return torch.nn.functional.silu(x)
def _sigmoid(x):
    return torch.sigmoid(x)
def _layernorm(x, axis=-1, eps=1e-5):
    m = x.mean(dim=axis, keepdim=True)
    v = x.var(dim=axis, keepdim=True, unbiased=False)
    return (x - m) / torch.sqrt(v + eps)
def _scatter_add(tgt, idx, src, axis=0):
    return tgt.clone().index_add_(axis, idx, src)
def _segment_sum(src, seg, n):
    out = torch.zeros((n,) + src.shape[1:], dtype=src.dtype)
    return out.index_add_(0, seg, src)
def _pad(x, pw):
    flat = tuple(v for pair in reversed(pw) for v in pair)
    return torch.nn.functional.pad(x, flat)

kernel_name = 'recurrent_scan__linear_attn_block__glu_gated_geglu__prompt'

def setup_inputs():

    _w = (lambda a: (lambda t: t.to(torch.bfloat16) if t.is_floating_point() else t)(torch.as_tensor(a)))
    def _A_setup():
        B, S, D = 1, 2048, 512
        return {
            "x": _w((lambda *s: torch.randn(*s, dtype=torch.bfloat16) * 0.05)(B, S, D)),
            "wqkv": _w((lambda *s: torch.randn(*s, dtype=torch.bfloat16) * 0.8)(D, 3 * D)),
            "wo": _w((lambda *s: torch.randn(*s, dtype=torch.bfloat16) * 0.8)(D, D)),
        }
    _A_in = _A_setup()

    def _B_setup():
        B, S, Dm, Df = 1, 2048, 512, 1376
        return {
            "x": _w((lambda *s: torch.randn(*s, dtype=torch.bfloat16) * 0.05)(B, S, Dm)),
            "w_gate": _w((lambda *s: torch.randn(*s, dtype=torch.bfloat16) * 0.2)(Dm, Df) * (Dm ** -0.5)),
            "w_up": _w((lambda *s: torch.randn(*s, dtype=torch.bfloat16) * 0.2)(Dm, Df) * (Dm ** -0.5)),
            "w_down": _w((lambda *s: torch.randn(*s, dtype=torch.bfloat16) * 0.2)(Df, Dm) * (Df ** -0.5)),
        }
    _B_in = _B_setup()

    return {
        "A_x": _A_in["x"],
        "A_wqkv": _A_in["wqkv"],
        "A_wo": _A_in["wo"],
        "B_x": _B_in["x"],
        "B_w_gate": _B_in["w_gate"],
        "B_w_up": _B_in["w_up"],
        "B_w_down": _B_in["w_down"],
    }

def reference(A_x, A_wqkv, A_wo, B_x, B_w_gate, B_w_up, B_w_down):
    def _A_ref(x, wqkv, wo):
        B, S, D = x.shape
        H = 32; Dh = D // H
        qkv = (lambda x, *d: x.permute(*d))((x @ wqkv).reshape(B, S, 3, H, Dh), 2, 0, 3, 1, 4)
        q, k, v = qkv[0], qkv[1], qkv[2]
        phi = lambda t: torch.where(t > 0, t + 1, torch.exp(t)) + 1e-6
        pq, pk = phi(q), phi(k)
        kv = torch.einsum("bhsd,bhse->bhde", pk, v)
        z = torch.einsum("bhsd->bhd", pk)
        num = torch.einsum("bhqd,bhde->bhqe", pq, kv)
        den = torch.einsum("bhqd,bhd->bhq", pq, z)[..., None] + 1e-6
        o = num / den
        return (lambda x, *d: x.permute(*d))(o, 0, 2, 1, 3).reshape(B, S, D) @ wo

    def _B_ref(x, w_gate, w_up, w_down):
        g = torch.einsum("bsd,df->bsf", x, w_gate)
        u = torch.einsum("bsd,df->bsf", x, w_up)
        h = _gelu(g) * u
        return torch.einsum("bsf,fd->bsd", h, w_down)

    x = A_x
    xn = _layernorm(x, axis=-1)
    s = _A_ref(xn, A_wqkv, A_wo)
    g = _silu(_B_ref(xn, B_w_gate, B_w_up, B_w_down))
    return x + s * g
```

```cpp
#include <hip/hip_runtime.h>
#include <hip/hip_bf16.h>
typedef unsigned short u16;
constexpr int S=2048, D=512, DF=1376, H=32, DH=16;
__device__ __forceinline__ float bf2f(u16 v){ return __uint_as_float(((unsigned)v)<<16); }
__device__ __forceinline__ u16 f2bf(float f){ __hip_bfloat16 b = __float2bfloat16(f); return *(u16*)&b; }
__device__ __forceinline__ float rb(float f){ return bf2f(f2bf(f)); }

__global__ void ln_k(const u16* x, u16* xn){
  int r = blockIdx.x; int t = threadIdx.x;
  float v[8]; float s=0;
  for(int i=0;i<8;i++){ v[i]=bf2f(x[r*D+t*8+i]); s+=v[i]; }
  for(int o=32;o>0;o>>=1) s+=__shfl_xor(s,o);
  float m = s/D; float q=0;
  for(int i=0;i<8;i++){ float d=v[i]-m; q+=d*d; }
  for(int o=32;o>0;o>>=1) q+=__shfl_xor(q,o);
  float mb = rb(m), vb = rb(q/D);
  float den = rb(sqrtf(rb(vb+1e-5f)));
  for(int i=0;i<8;i++) xn[r*D+t*8+i]=f2bf(rb(v[i]-mb)/den);
}
__global__ void gemm_k(const u16* A, const u16* B, u16* C, int M, int N, int K){
  __shared__ float As[32][33], Bs[32][33];
  int tx=threadIdx.x&31, ty=threadIdx.x>>5;
  int row=blockIdx.y*32+ty, col=blockIdx.x*32+tx; float acc=0;
  for(int k0=0;k0<K;k0+=32){
    As[ty][tx]=bf2f(A[row*K+k0+tx]); Bs[ty][tx]=bf2f(B[(k0+ty)*N+col]);
    __syncthreads();
    for(int k=0;k<32;k++) acc+=As[ty][k]*Bs[k][tx];
    __syncthreads();
  }
  C[row*N+col]=f2bf(acc);
}
__device__ __forceinline__ float phi(float t){ float r = t>0? rb(t+1.f) : rb(expf(t)); return rb(r+1e-6f); }
__global__ void kv_k(const u16* qkv, float* kv, float* z){
  int h=blockIdx.x; int d=threadIdx.x>>4, e=threadIdx.x&15; float a=0, zz=0;
  for(int s=0;s<S;s++){ float pk=phi(bf2f(qkv[s*3*D+D+h*DH+d])); a+=pk*bf2f(qkv[s*3*D+2*D+h*DH+e]); zz+=pk; }
  kv[h*256+d*16+e]=rb(a); if(e==0) z[h*16+d]=rb(zz);
}
__global__ void o_k(const u16* qkv, const float* kv, const float* z, u16* o){
  int s=blockIdx.x; int h=threadIdx.x>>4, e=threadIdx.x&15; float num=0, den=0;
  for(int d=0;d<16;d++){ float pq=phi(bf2f(qkv[s*3*D+h*DH+d])); num+=pq*kv[h*256+d*16+e]; den+=pq*z[h*16+d]; }
  o[s*D+h*16+e]=f2bf(rb(num)/rb(rb(den)+1e-6f));
}
__global__ void h_k(const u16* g, const u16* u, u16* h){
  int i=blockIdx.x*256+threadIdx.x; float x=bf2f(g[i]);
  float ge=0.5f*x*(1.f+tanhf(0.7978845608f*(x+0.044715f*x*x*x)));
  h[i]=f2bf(rb(ge)*bf2f(u[i]));
}
__global__ void fin_k(const u16* x, const u16* s, const u16* f, u16* out){
  int i=blockIdx.x*256+threadIdx.x; float ff=bf2f(f[i]); float g=rb(ff/(1.f+expf(-ff)));
  out[i]=f2bf(bf2f(x[i])+rb(bf2f(s[i])*g));
}
extern "C" void kernel_launch(void* const* d_in, const int* in_sizes, int n_in,
                              void* d_out, int out_size, void* d_ws, size_t ws_size,
                              hipStream_t stream) {
  const u16* x=(const u16*)d_in[0]; const u16* wqkv=(const u16*)d_in[1]; const u16* wo=(const u16*)d_in[2];
  const u16* wg=(const u16*)d_in[4]; const u16* wu=(const u16*)d_in[5]; const u16* wd=(const u16*)d_in[6];
  char* ws=(char*)d_ws;
  u16* xn=(u16*)ws; u16* qkv=(u16*)(ws+(4<<20)); u16* g=(u16*)(ws+(12<<20)); u16* u=(u16*)(ws+(20<<20));
  u16* h=(u16*)(ws+(28<<20)); u16* o=(u16*)(ws+(36<<20)); u16* s=(u16*)(ws+(40<<20)); u16* f=(u16*)(ws+(44<<20));
  float* kv=(float*)(ws+(48<<20)); float* z=(float*)(ws+(49<<20));
  ln_k<<<S,64,0,stream>>>(x,xn);
  gemm_k<<<dim3(3*D/32,S/32),1024,0,stream>>>(xn,wqkv,qkv,S,3*D,D);
  gemm_k<<<dim3(DF/32,S/32),1024,0,stream>>>(xn,wg,g,S,DF,D);
  gemm_k<<<dim3(DF/32,S/32),1024,0,stream>>>(xn,wu,u,S,DF,D);
  kv_k<<<H,256,0,stream>>>(qkv,kv,z);
  o_k<<<S,512,0,stream>>>(qkv,kv,z,o);
  h_k<<<S*DF/256,256,0,stream>>>(g,u,h);
  gemm_k<<<dim3(D/32,S/32),1024,0,stream>>>(o,wo,s,S,D,D);
  gemm_k<<<dim3(D/32,S/32),1024,0,stream>>>(h,wd,f,S,D,DF);
  fin_k<<<S*D/256,256,0,stream>>>(x,s,f,(u16*)d_out);
}
```

```cpp
#include <hip/hip_runtime.h>
#include <hip/hip_bf16.h>
typedef unsigned short u16;
constexpr int S=2048, D=512, DF=1376, H=32, DH=16;
__device__ __forceinline__ float bf2f(u16 v){ return __uint_as_float(((unsigned)v)<<16); }
__device__ __forceinline__ u16 f2bf(float f){ __hip_bfloat16 b = __float2bfloat16(f); return *(u16*)&b; }
__device__ __forceinline__ float rb(float f){ return bf2f(f2bf(f)); }

__global__ void ln_k(const u16* x, u16* xn){
  int r = blockIdx.x; int t = threadIdx.x;
  float v[8]; float s=0;
  for(int i=0;i<8;i++){ v[i]=bf2f(x[r*D+t*8+i]); s+=v[i]; }
  for(int o=32;o>0;o>>=1) s+=__shfl_xor(s,o);
  float m = s/D; float q=0;
  for(int i=0;i<8;i++){ float d=v[i]-m; q+=d*d; }
  for(int o=32;o>0;o>>=1) q+=__shfl_xor(q,o);
  float mb = rb(m), vb = rb(q/D);
  float den = rb(sqrtf(rb(vb+1e-5f)));
  for(int i=0;i<8;i++) xn[r*D+t*8+i]=f2bf(rb(v[i]-mb)/den);
}
__global__ void gemm_k(const u16* A, const u16* B, u16* C, int M, int N, int K){
  __shared__ float As[32][33], Bs[32][33];
  int tx=threadIdx.x&31, ty=threadIdx.x>>5;
  int row=blockIdx.y*32+ty, col=blockIdx.x*32+tx; float acc=0;
  for(int k0=0;k0<K;k0+=32){
    As[ty][tx]=bf2f(A[row*K+k0+tx]); Bs[ty][tx]=bf2f(B[(k0+ty)*N+col]);
    __syncthreads();
    for(int k=0;k<32;k++) acc+=As[ty][k]*Bs[k][tx];
    __syncthreads();
  }
  C[row*N+col]=f2bf(acc);
}
__device__ __forceinline__ float phi(float t){ float r = t>0? rb(t+1.f) : rb(expf(t)); return rb(r+1e-6f); }
__global__ void kv_k(const u16* qkv, float* kv, float* z){
  int h=blockIdx.x; int d=threadIdx.x>>4, e=threadIdx.x&15; float a=0, zz=0;
  for(int s=0;s<S;s++){ float pk=phi(bf2f(qkv[s*3*D+D+h*DH+d])); a+=pk*bf2f(qkv[s*3*D+2*D+h*DH+e]); zz+=pk; }
  kv[h*256+d*16+e]=rb(a); if(e==0) z[h*16+d]=rb(zz);
}
__global__ void o_k(const u16* qkv, const float* kv, const float* z, u16* o){
  int s=blockIdx.x; int h=threadIdx.x>>4, e=threadIdx.x&15; float num=0, den=0;
  for(int d=0;d<16;d++){ float pq=phi(bf2f(qkv[s*3*D+h*DH+d])); num+=pq*kv[h*256+d*16+e]; den+=pq*z[h*16+d]; }
  o[s*D+h*16+e]=f2bf(rb(num)/rb(rb(den)+1e-6f));
}
__global__ void h_k(const u16* g, const u16* u, u16* h){
  int i=blockIdx.x*256+threadIdx.x; float x=bf2f(g[i]);
  float ge=0.5f*x*(1.f+tanhf(0.7978845608f*(x+0.044715f*x*x*x)));
  h[i]=f2bf(rb(ge)*bf2f(u[i]));
}
__global__ void fin_k(const u16* x, const u16* s, const u16* f, u16* out){
  int i=blockIdx.x*256+threadIdx.x; float ff=bf2f(f[i]); float g=rb(ff/(1.f+expf(-ff)));
  out[i]=f2bf(bf2f(x[i])+rb(bf2f(s[i])*g));
}

__global__ void probe_k(const char* p, int n, int stride, unsigned* sink){
  unsigned acc=0;
  for(int i=0;i<n;i++){
    unsigned v;
    const char* a = p + (size_t)i*stride;
    asm volatile("global_load_dword %0, %1, off\n s_waitcnt vmcnt(0)":"=v"(v):"v"(a):"memory");
    acc+=v;
  }
  sink[0]=acc;
}
extern "C" void kernel_launch(void* const* d_in, const int* in_sizes, int n_in,
                              void* d_out, int out_size, void* d_ws, size_t ws_size,
                              hipStream_t stream) {
  const u16* x=(const u16*)d_in[0]; const u16* wqkv=(const u16*)d_in[1]; const u16* wo=(const u16*)d_in[2];
  const u16* wg=(const u16*)d_in[4]; const u16* wu=(const u16*)d_in[5]; const u16* wd=(const u16*)d_in[6];
  char* ws=(char*)d_ws;
  u16* xn=(u16*)ws; u16* qkv=(u16*)(ws+(4<<20)); u16* g=(u16*)(ws+(12<<20)); u16* u=(u16*)(ws+(20<<20));
  u16* h=(u16*)(ws+(28<<20)); u16* o=(u16*)(ws+(36<<20)); u16* s=(u16*)(ws+(40<<20)); u16* f=(u16*)(ws+(44<<20));
  float* kv=(float*)(ws+(48<<20)); float* z=(float*)(ws+(49<<20));
  probe_k<<<1,1,0,stream>>>((const char*)d_in[0],0,1024,(unsigned*)(ws+(50<<20)));
  ln_k<<<S,64,0,stream>>>(x,xn);
  gemm_k<<<dim3(3*D/32,S/32),1024,0,stream>>>(xn,wqkv,qkv,S,3*D,D);
  gemm_k<<<dim3(DF/32,S/32),1024,0,stream>>>(xn,wg,g,S,DF,D);
  gemm_k<<<dim3(DF/32,S/32),1024,0,stream>>>(xn,wu,u,S,DF,D);
  kv_k<<<H,256,0,stream>>>(qkv,kv,z);
  o_k<<<S,512,0,stream>>>(qkv,kv,z,o);
  h_k<<<S*DF/256,256,0,stream>>>(g,u,h);
  gemm_k<<<dim3(D/32,S/32),1024,0,stream>>>(o,wo,s,S,D,D);
  gemm_k<<<dim3(D/32,S/32),1024,0,stream>>>(h,wd,f,S,D,DF);
  fin_k<<<S*D/256,256,0,stream>>>(x,s,f,(u16*)d_out);
}
```

```cpp
#include <hip/hip_runtime.h>
#include <hip/hip_bf16.h>
typedef unsigned short u16;
constexpr int S=2048, D=512, DF=1376, H=32, DH=16;
__device__ __forceinline__ float bf2f(u16 v){ return __uint_as_float(((unsigned)v)<<16); }
__device__ __forceinline__ u16 f2bf(float f){ __hip_bfloat16 b = __float2bfloat16(f); return *(u16*)&b; }
__device__ __forceinline__ float rb(float f){ return bf2f(f2bf(f)); }

__global__ void ln_k(const u16* x, u16* xn){
  int r = blockIdx.x; int t = threadIdx.x;
  float v[8]; float s=0;
  for(int i=0;i<8;i++){ v[i]=bf2f(x[r*D+t*8+i]); s+=v[i]; }
  for(int o=32;o>0;o>>=1) s+=__shfl_xor(s,o);
  float m = s/D; float q=0;
  for(int i=0;i<8;i++){ float d=v[i]-m; q+=d*d; }
  for(int o=32;o>0;o>>=1) q+=__shfl_xor(q,o);
  float mb = rb(m), vb = rb(q/D);
  float den = rb(sqrtf(rb(vb+1e-5f)));
  for(int i=0;i<8;i++) xn[r*D+t*8+i]=f2bf(rb(v[i]-mb)/den);
}
__global__ void gemm_k(const u16* A, const u16* B, u16* C, int M, int N, int K){
  __shared__ float As[32][33], Bs[32][33];
  int tx=threadIdx.x&31, ty=threadIdx.x>>5;
  int row=blockIdx.y*32+ty, col=blockIdx.x*32+tx; float acc=0;
  for(int k0=0;k0<K;k0+=32){
    As[ty][tx]=bf2f(A[row*K+k0+tx]); Bs[ty][tx]=bf2f(B[(k0+ty)*N+col]);
    __syncthreads();
    for(int k=0;k<32;k++) acc+=As[ty][k]*Bs[k][tx];
    __syncthreads();
  }
  C[row*N+col]=f2bf(acc);
}
__device__ __forceinline__ float phi(float t){ float r = t>0? rb(t+1.f) : rb(expf(t)); return rb(r+1e-6f); }
__global__ void kv_k(const u16* qkv, float* kv, float* z){
  int h=blockIdx.x; int d=threadIdx.x>>4, e=threadIdx.x&15; float a=0, zz=0;
  for(int s=0;s<S;s++){ float pk=phi(bf2f(qkv[s*3*D+D+h*DH+d])); a+=pk*bf2f(qkv[s*3*D+2*D+h*DH+e]); zz+=pk; }
  kv[h*256+d*16+e]=rb(a); if(e==0) z[h*16+d]=rb(zz);
}
__global__ void o_k(const u16* qkv, const float* kv, const float* z, u16* o){
  int s=blockIdx.x; int h=threadIdx.x>>4, e=threadIdx.x&15; float num=0, den=0;
  for(int d=0;d<16;d++){ float pq=phi(bf2f(qkv[s*3*D+h*DH+d])); num+=pq*kv[h*256+d*16+e]; den+=pq*z[h*16+d]; }
  o[s*D+h*16+e]=f2bf(rb(num)/rb(rb(den)+1e-6f));
}
__global__ void h_k(const u16* g, const u16* u, u16* h){
  int i=blockIdx.x*256+threadIdx.x; float x=bf2f(g[i]);
  float ge=0.5f*x*(1.f+tanhf(0.7978845608f*(x+0.044715f*x*x*x)));
  h[i]=f2bf(rb(ge)*bf2f(u[i]));
}
__global__ void fin_k(const u16* x, const u16* s, const u16* f, u16* out){
  int i=blockIdx.x*256+threadIdx.x; float ff=bf2f(f[i]); float g=rb(ff/(1.f+expf(-ff)));
  out[i]=f2bf(bf2f(x[i])+rb(bf2f(s[i])*g));
}

typedef __attribute__((ext_vector_type(4))) unsigned u32x4;
__global__ __launch_bounds__(512) void probe_k(const u32x4* p, int n, unsigned* sink){
  unsigned acc=0;
  const u32x4* a = p + (size_t)blockIdx.x*n*512 + threadIdx.x;
  for(int i=0;i<n;i+=8){
    u32x4 v[8];
    #pragma unroll
    for(int j=0;j<8;j++) v[j] = a[(i+j)*512];
    #pragma unroll
    for(int j=0;j<8;j++) acc^=v[j][0]^v[j][1]^v[j][2]^v[j][3];
  }
  if(acc==0x12345678) sink[0]=acc;
}
extern "C" void kernel_launch(void* const* d_in, const int* in_sizes, int n_in,
                              void* d_out, int out_size, void* d_ws, size_t ws_size,
                              hipStream_t stream) {
  const u16* x=(const u16*)d_in[0]; const u16* wqkv=(const u16*)d_in[1]; const u16* wo=(const u16*)d_in[2];
  const u16* wg=(const u16*)d_in[4]; const u16* wu=(const u16*)d_in[5]; const u16* wd=(const u16*)d_in[6];
  char* ws=(char*)d_ws;
  u16* xn=(u16*)ws; u16* qkv=(u16*)(ws+(4<<20)); u16* g=(u16*)(ws+(12<<20)); u16* u=(u16*)(ws+(20<<20));
  u16* h=(u16*)(ws+(28<<20)); u16* o=(u16*)(ws+(36<<20)); u16* s=(u16*)(ws+(40<<20)); u16* f=(u16*)(ws+(44<<20));
  float* kv=(float*)(ws+(48<<20)); float* z=(float*)(ws+(49<<20));
  probe_k<<<256,512,0,stream>>>((const u32x4*)(ws+(64<<20)),0,(unsigned*)(ws+(50<<20)));
  ln_k<<<S,64,0,stream>>>(x,xn);
  gemm_k<<<dim3(3*D/32,S/32),1024,0,stream>>>(xn,wqkv,qkv,S,3*D,D);
  gemm_k<<<dim3(DF/32,S/32),1024,0,stream>>>(xn,wg,g,S,DF,D);
  gemm_k<<<dim3(DF/32,S/32),1024,0,stream>>>(xn,wu,u,S,DF,D);
  kv_k<<<H,256,0,stream>>>(qkv,kv,z);
  o_k<<<S,512,0,stream>>>(qkv,kv,z,o);
  h_k<<<S*DF/256,256,0,stream>>>(g,u,h);
  gemm_k<<<dim3(D/32,S/32),1024,0,stream>>>(o,wo,s,S,D,D);
  gemm_k<<<dim3(D/32,S/32),1024,0,stream>>>(h,wd,f,S,D,DF);
  fin_k<<<S*D/256,256,0,stream>>>(x,s,f,(u16*)d_out);
}
```

```cpp
#include <hip/hip_runtime.h>

typedef unsigned long long u64;
typedef __attribute__((ext_vector_type(8))) __bf16 bf16x8;
typedef __attribute__((ext_vector_type(2))) __bf16 bf16x2;
typedef __attribute__((ext_vector_type(2))) float f32x2;
typedef __attribute__((ext_vector_type(4))) float f32x4;
typedef __attribute__((ext_vector_type(16))) float f32x16;
typedef __attribute__((ext_vector_type(2))) unsigned u32x2;
typedef __attribute__((ext_vector_type(4))) unsigned u32x4;
typedef __attribute__((ext_vector_type(4))) int i32x4;
typedef __attribute__((ext_vector_type(4))) short s16x4;
#define DI __device__ __forceinline__
#define LAS __attribute__((address_space(3)))
#define GAS __attribute__((address_space(1)))
#define SB() __builtin_amdgcn_sched_barrier(0)
#define CFENCE() asm volatile("" ::: "memory")

#define ROT_MODE 2
#define SHIFT_N 8

constexpr int SEQ = 2048, DM = 512, DF = 1376;
constexpr int NSTEP = 59;
constexpr unsigned WS_EPOCH = 0;
constexpr unsigned WS_FH = 4096;
constexpr unsigned WS_FK = WS_FH + 256 * 128;
constexpr unsigned WS_FO = WS_FK + 128 * 128;
constexpr unsigned WS_KP = 131072;
constexpr unsigned WS_ZP = WS_KP + 8 * 16 * 4096;
constexpr unsigned WS_ACT = 1048576;
constexpr unsigned WS_END = WS_ACT + 128 * NSTEP * 1024;
constexpr unsigned WS_DBG = 16u << 20;
constexpr unsigned SLOT = 53376;
constexpr unsigned L_W2STEPS = 34;
constexpr unsigned L_SCR = L_W2STEPS * 4096;
constexpr unsigned L_ST = L_SCR;
constexpr unsigned L_CS = L_SCR + 4096;
constexpr unsigned L_RS = L_SCR + 6656;
constexpr unsigned L_KX = L_SCR + 8192;
constexpr unsigned L_FX = L_SCR;
constexpr unsigned L_SX = 26 * 4096;
constexpr unsigned L_CTL = 3 * SLOT;
enum { C_H = 0, C_K, C_O, C_KVX0, C_KVX1, C_KVR0, C_KVR1, C_W0, C_RH1, C_RH2, C_RO, C_F1, C_F2, C_F3, C_W1, C_W2, C_P0  , C_N = C_P0 + 8, C_TAG = 32 };
constexpr unsigned LDS_BYTES = 163840;
constexpr unsigned SPIN_CAP = 1u << 20;

struct Args {
  const void* x; const void* wqkv; const void* wo; const void* wg; const void* wu; const void* wd; void* out; void* ws;
};

DI bf16x8 asbf(u32x4 v) { return __builtin_bit_cast(bf16x8, v); }
DI f32x16 mfma32(u32x4 a, u32x4 b, f32x16 c) { return __builtin_amdgcn_mfma_f32_32x32x16_bf16(asbf(a), asbf(b), c, 0, 0, 0); }
DI f32x4 mfma16(u32x4 a, u32x4 b, f32x4 c) { return __builtin_amdgcn_mfma_f32_16x16x32_bf16(asbf(a), asbf(b), c, 0, 0, 0); }
DI unsigned pk2(float a, float b) { f32x2 v = {a, b}; return __builtin_bit_cast(unsigned, __builtin_convertvector(v, bf16x2)); }
DI float rbf(float a) { return __uint_as_float(pk2(a, a) << 16); }
DI float bflo(unsigned w) { return __uint_as_float(w << 16); }
DI float bfhi(unsigned w) { return __uint_as_float(w & 0xffff0000u); }
DI u32x4 lds128(unsigned a) { return *(const LAS u32x4*)(size_t)a; }
DI f32x4 lds128f(unsigned a) { return *(const LAS f32x4*)(size_t)a; }
DI u32x2 ldstr(unsigned a) { return __builtin_bit_cast(u32x2, __builtin_amdgcn_ds_read_tr16_b64_v4i16((LAS s16x4*)(size_t)a)); }
DI u32x4 ldsfrag(unsigned a0, unsigned a1) { u32x2 l = ldstr(a0), h = ldstr(a1); return u32x4{l[0], l[1], h[0], h[1]}; }
DI i32x4 mksrd(const void* p, unsigned bytes) {
  u64 a = (u64)p; return i32x4{(int)(unsigned)a, (int)((unsigned)(a >> 32) & 0xffffu), (int)bytes, 0x00020000};
}
DI void dma(unsigned lds, unsigned voff, i32x4 srd, unsigned soff) {
  asm volatile("s_mov_b32 m0, %0\n\ts_nop 0\n\tbuffer_load_dwordx4 %1, %2, %3 offen lds" ::"s"(lds), "v"(voff), "s"(srd), "s"(soff) : "memory");
}
template <int N> DI void wait_vm() { asm volatile("s_waitcnt vmcnt(%0)" ::"i"(N) : "memory"); }
DI void bar() { asm volatile("s_waitcnt lgkmcnt(0)\n\ts_barrier" ::: "memory"); }
DI LAS unsigned* ctl(int i) { return (LAS unsigned*)(size_t)(L_CTL + 4 * i); }
DI u64 get_tag() { return *(const LAS u64*)(size_t)(L_CTL + 4 * C_TAG); }
DI void lds_wait(int i, unsigned want) {
  CFENCE();
#pragma unroll 1
  for (unsigned s = 0; s < SPIN_CAP; ++s) {
    unsigned v = __builtin_amdgcn_readfirstlane(__hip_atomic_load(ctl(i), __ATOMIC_RELAXED, __HIP_MEMORY_SCOPE_WORKGROUP));
    if (v >= want) break;
    __builtin_amdgcn_s_sleep(1);
  }
  CFENCE();
}
DI void lds_inc(int i, int lane) {
  CFENCE();
  if (lane == 0) __hip_atomic_fetch_add(ctl(i), 1u, __ATOMIC_RELAXED, __HIP_MEMORY_SCOPE_WORKGROUP);
  CFENCE();
}
DI void publish(int i, unsigned n, int lane, char* ws, unsigned flag_off, u64 tag) {
  wait_vm<0>();
  if (lane == 0) {
    unsigned old = __hip_atomic_fetch_add(ctl(i), 1u, __ATOMIC_RELAXED, __HIP_MEMORY_SCOPE_WORKGROUP);
    if (old == n - 1) __hip_atomic_store((GAS u64*)(ws + flag_off), tag, __ATOMIC_RELAXED, __HIP_MEMORY_SCOPE_AGENT);
  }
  CFENCE();
}
DI void poll(char* ws, unsigned off, u64 tag) {
  CFENCE();
#pragma unroll 1
  for (unsigned s = 0; s < SPIN_CAP; ++s) {
    u64 v = __hip_atomic_load((GAS u64*)(ws + off), __ATOMIC_RELAXED, __HIP_MEMORY_SCOPE_AGENT);
    if (__all(v == tag)) break;
    __builtin_amdgcn_s_sleep(2);
  }
  CFENCE();
}

#define TS(i)
#define TS_DECL
#define TS_ARG
#define TS_PASS

struct Geo { int rt, g8, u0, rot, tb64, nt; };

template <int ROLE> struct Ld {
  static constexpr int N = ROLE ? 13 : 12;
  i32x4 sx, sa, sg, su;
  unsigned vx0, vx1, va, vg[7], vu;
  unsigned xo, ao, go;
  int lw, rot;
  template <int I> DI void one(int s) const {
    const unsigned k0 = ((unsigned)(s + rot) & 7u) * 64u;
    const unsigned sl = (unsigned)(s % 3) * SLOT;
    if constexpr (I < 4) {
      dma(sl + (4 * lw + I) * 1024, (I & 1) ? vx1 : vx0, sx, xo + I * 8192 + k0 * 2);
    } else if constexpr (ROLE == 1) {
      if constexpr (I < 6) dma(sl + 16384 + (2 * lw + I - 4) * 1024, va, sa, ao + (k0 + 8 * (I - 4)) * 3072);
      else dma(sl + 24576 + (lw >> 1) * 14464 + (lw & 1) * 7168 + (I - 6) * 1024, vg[I - 6], sg, go + k0 * 2752);
    } else {
      constexpr int M = (I - 4) >> 1, J = I & 1;
      if constexpr (M < 2) dma(sl + 16384 + M * 8192 + (2 * lw + J) * 1024, va, sa, ao + M * 1024 + (k0 + 8 * J) * 3072);
      else if constexpr (M == 2) dma(sl + 32768 + (2 * lw + J) * 1024, vg[0], sg, go + (k0 + 8 * J) * 2752);
      else dma(sl + 40960 + (2 * lw + J) * 1024, vu, su, go + (k0 + 8 * J) * 2752);
    }
  }
  template <int I0, int I1> DI void range(int s) const {
    if constexpr (I0 < I1) { one<I0>(s); range<I0 + 1, I1>(s); }
  }
};

template <int ROLE> DI void loader(const Args& a, const Geo& g, int lw, int lane TS_ARG) {
  Ld<ROLE> L;
  constexpr int N = Ld<ROLE>::N, SH = SHIFT_N < N ? SHIFT_N : N;
  const unsigned r8 = lane >> 3, c8 = lane & 7;
  L.lw = lw; L.rot = g.rot;
  L.sx = mksrd(a.x, SEQ * DM * 2);
  L.sa = mksrd(a.wqkv, DM * 3 * DM * 2);
  L.vx0 = r8 * 1024 + (c8 ^ (r8 >> 1)) * 16;
  L.vx1 = r8 * 1024 + (c8 ^ ((r8 >> 1) + 4)) * 16;
  L.va = r8 * 3072 + (c8 ^ ((r8 & 2) << 1)) * 16;
  L.xo = (g.rt * 128 + 32 * lw) * 1024;
  if constexpr (ROLE == 1) {
    L.sg = mksrd(lw < 2 ? a.wg : a.wu, DM * DF * 2); L.su = L.sg;
    L.ao = g.g8 * 128 + 16 * lw * 3072;
    L.go = g.u0 * 32 + (lw & 1) * 32 * 2752;
#pragma unroll
    for (int i = 0; i < 7; ++i) { unsigned idx = 64 * i + lane, row = idx / 14; L.vg[i] = row * 2752 + (idx - row * 14) * 16; }
    L.vu = 0;
  } else {
    L.sg = mksrd(a.wg, DM * DF * 2); L.su = mksrd(a.wu, DM * DF * 2);
    L.ao = 1024 + g.g8 * 128 + 16 * lw * 3072;
    L.go = g.u0 * 32 + 16 * lw * 2752;
    L.vg[0] = r8 * 2752 + (c8 ^ ((r8 & 2) << 1)) * 16;
    L.vu = r8 * 2752 + (c8 ^ ((r8 & 2) << 1) ^ 2) * 16;
#pragma unroll
    for (int i = 1; i < 7; ++i) L.vg[i] = 0;
  }
  const i32x4 sd = mksrd(a.wd, DF * DM * 2), so = mksrd(a.wo, DM * DM * 2);
  const unsigned vw = r8 * 1024 + (c8 ^ (((r8 >> 1) & 3) << 1)) * 16;
  auto w2 = [&](int j, int q, int r) {
    const bool d = j < 43;
    i32x4 s; s[0] = d ? sd[0] : so[0]; s[1] = d ? sd[1] : so[1]; s[2] = d ? sd[2] : so[2]; s[3] = sd[3];
    dma(r * 4096 + q * 1024, vw, s, ((d ? j : j - 43) * 32 + 8 * q) * 1024 + g.nt * 128);
  };
  TS(0);
  L.template range<0, N>(0);
  L.template range<0, SH>(1);
#pragma unroll 1
  for (int s = 0; s < 6; ++s) {
    wait_vm<SH>(); bar();
    L.template range<SH, N>(s + 1);
    L.template range<0, SH>(s + 2);
  }
  wait_vm<SH>(); bar();
  TS(1);
  L.template range<SH, N>(7);
  _Pragma("unroll 1") for (int j = 27; j < 34; ++j) w2(j, lw, j);
  wait_vm<7>(); bar();
  TS(2);
  _Pragma("unroll 1") for (int j = 0; j < 9; ++j) w2(j, lw, j);
  bar();
  TS(3);
  _Pragma("unroll 1") for (int j = 9; j < 27; ++j) w2(j, lw, j);
  wait_vm<0>(); lds_inc(C_W0, lane);
  TS(4);
  char* ws = (char*)a.ws;
  if (lw == 3) {
    const u64 tag = get_tag();
    const int rt2 = g.tb64 >> 1;
    poll(ws, WS_FH + (rt2 * 16 + (lane & 7)) * 128, tag); lds_inc(C_RH1, lane);
    TS(5);
    poll(ws, WS_FH + (rt2 * 16 + 8 + (lane & 7)) * 128, tag); lds_inc(C_RH2, lane);
    TS(6);
    poll(ws, WS_FO + (rt2 * 8 + (lane & 7)) * 128, tag); lds_inc(C_RO, lane);
    TS(7);
    if (blockIdx.x == 0) {
      _Pragma("unroll 1") for (int q = 0; q < 4; ++q) poll(ws, WS_FH + (q * 64 + lane) * 128, tag);
      if (lane == 0) __hip_atomic_store((GAS u64*)(ws + WS_EPOCH), tag, __ATOMIC_RELAXED, __HIP_MEMORY_SCOPE_AGENT);
    }
  } else {
    lds_wait(C_F1, 8);
    TS(5);
    _Pragma("unroll 1") for (int i = lw; i < 48; i += 3) w2(34 + (i >> 2), i & 3, i >> 2);
    wait_vm<0>(); lds_inc(C_W1, lane);
    TS(6);
    lds_wait(C_F2, 8);
    _Pragma("unroll 1") for (int i = lw; i < 52; i += 3) w2(46 + (i >> 2), i & 3, 12 + (i >> 2));
    wait_vm<0>(); lds_inc(C_W2, lane);
    TS(7);
  }
}

struct TokC { float c0, c1, kk, rs; };

DI void st128(__amdgpu_buffer_rsrc_t rw, unsigned off, u32x4 v) { __builtin_amdgcn_raw_buffer_store_b128(v, rw, off, 0, 16); }
DI void store_act(__amdgpu_buffer_rsrc_t rw, unsigned voff, int t32, int unit, u32x4 v) {
  st128(rw, voff + (WS_ACT + (2 * t32 * NSTEP + (unit >> 1)) * 1024 + (unit & 1) * 512), v);
}
DI u32x4 geglu(const f32x16& acc, const TokC& t) {
  float h[8];
#pragma unroll
  for (int i = 0; i < 8; ++i) {
    const float gq = acc[i], uq = acc[i + 8];
    const float e = __builtin_amdgcn_exp2f(gq * __builtin_fmaf(gq * gq, t.c1, t.c0));
    h[i] = (gq * uq) * __builtin_amdgcn_rcpf(__builtin_fmaf(e, t.kk, t.kk));
  }
  return u32x4{pk2(h[0], h[1]), pk2(h[2], h[3]), pk2(h[4], h[5]), pk2(h[6], h[7])};
}
DI float phi(float t) {
  return (__builtin_fmaxf(t, 0.f) + 1e-6f) + __builtin_amdgcn_exp2f(__builtin_fminf(t, 0.f) * 1.44269504f);
}

template <int ROLE, int TYPE> DI void phase1(const Args& a, const Geo& g, int w, int lane TS_ARG) {
  constexpr bool EX = ROLE == 1 && TYPE == 0, TOKROW = ROLE == 0 && TYPE == 0;
  constexpr unsigned P0 = (ROLE == 1 && TYPE == 1) ? 224 : 128, P1 = ROLE == 1 ? 224 : 128;
  const int m = (w >> 1) & 1, n = w & 1;
  const unsigned r = lane & 31, h = lane >> 5, blk = (lane >> 4) & 1, q = (lane >> 2) & 3, p = lane & 3;
  unsigned xa[4];
#pragma unroll
  for (int k = 0; k < 4; ++k) xa[k] = (64 * m + 32 * n + r) * 128 + (((2 * (k ^ (2 * m)) + h) ^ ((r >> 1) & 7)) * 16);
  const int xd = n ? -4096 : 4096;
  const unsigned krow = 8 * h + q;
  unsigned w0, w1, wx = 0;
  int id0, id1;
  if constexpr (ROLE == 0 && TYPE == 0) {
    w0 = 16384 + krow * 128 + ((2 * n + blk) ^ (q & 2)) * 32 + p * 8; w1 = w0 + 8192; id0 = n; id1 = 2 + n;
  } else if constexpr (ROLE == 0) {
    const unsigned b = 32768 + blk * 8192 + krow * 128 + p * 8;
    w0 = b + ((2 * n) ^ (q & 2) ^ blk) * 32; w1 = b + ((2 * n + 1) ^ (q & 2) ^ blk) * 32; id0 = 4 + 2 * n; id1 = 5 + 2 * n;
  } else {
    const unsigned b = 24576 + blk * 14464 + krow * 224 + p * 8;
    if constexpr (TYPE == 0) {
      w0 = 16384 + krow * 128 + ((2 * n + blk) ^ (q & 2)) * 32 + p * 8; w1 = b + n * 32; wx = b + 6 * 32; id0 = n; id1 = 2 + n;
    } else { w0 = b + (2 + 2 * n) * 32; w1 = w0 + 32; id0 = 4 + 2 * n; id1 = 5 + 2 * n; }
  }
  const unsigned o0a = m * 32 * P0, o0b = (m ^ 1) * 32 * P0, o1a = m * 32 * P1, o1b = (m ^ 1) * 32 * P1;
  const unsigned selw = ((((lane & 15) >> 2) & 1) == blk) ? 0x3f803f80u : 0u;
  const u32x4 sel = {selw, selw, selw, selw};

  f32x16 acc00 = {}, acc01 = {}, acc10 = {}, acc11 = {}, accx = {};
  f32x4 cs0 = {}, cs1 = {}, csx = {};
  float s0 = 0.f, s1 = 0.f, q0 = 0.f, q1 = 0.f;
  struct Fr { u32x4 x0, x1, w0, w1, wx; };
  Fr A, B;
  B.x0 = B.x1 = B.w0 = B.w1 = B.wx = u32x4{0, 0, 0, 0};
  auto ld = [&](Fr& f, unsigned sl, int k) {
    const unsigned a0 = sl + w0 + (k < 2 ? o0a : o0b) + (k & 1) * 16 * P0;
    const unsigned a1 = sl + w1 + (k < 2 ? o1a : o1b) + (k & 1) * 16 * P1;
    f.x0 = lds128(sl + xa[k]);
    f.w0 = ldsfrag(a0, a0 + 4 * P0);
    f.x1 = lds128(sl + xa[k] + xd);
    f.w1 = ldsfrag(a1, a1 + 4 * P1);
    if constexpr (EX) { const unsigned ax = sl + wx + (k < 2 ? o1a : o1b) + (k & 1) * 16 * P1; f.wx = ldsfrag(ax, ax + 4 * P1); }
  };
  auto mm = [&](const Fr& f, int k) {
    if constexpr (TOKROW) {
      acc00 = mfma32(f.x0, f.w0, acc00); acc01 = mfma32(f.x1, f.w0, acc01);
      acc10 = mfma32(f.x0, f.w1, acc10); acc11 = mfma32(f.x1, f.w1, acc11);
    } else {
      acc00 = mfma32(f.w0, f.x0, acc00); acc01 = mfma32(f.w0, f.x1, acc01);
      acc10 = mfma32(f.w1, f.x0, acc10); acc11 = mfma32(f.w1, f.x1, acc11);
      if constexpr (EX) accx = mfma32(f.wx, f.x0, accx);
    }
    if (k < 2) {
      cs0 = mfma16(sel, f.w0, cs0); cs1 = mfma16(sel, f.w1, cs1);
      if constexpr (EX) csx = mfma16(sel, f.wx, csx);
    }
    if ((k >> 1) == TYPE) {
#pragma unroll
      for (int i = 0; i < 4; ++i) {
        const float lo = bflo(f.x0[i]), hi = bfhi(f.x0[i]);
        s0 += lo; s1 += hi; q0 = __builtin_fmaf(lo, lo, q0); q1 = __builtin_fmaf(hi, hi, q1);
      }
    }
  };
  TS(0);
  unsigned sl = 0;
#pragma unroll 1
  for (int s = 0; s < 8; ++s) {
    bar();
    ld(A, sl, 0); SB(); mm(B, 3); SB();
    ld(B, sl, 1); SB(); mm(A, 0); SB();
    ld(A, sl, 2); SB(); mm(B, 1); SB();
    ld(B, sl, 3); SB(); mm(A, 2); SB();
    sl = sl == 2 * SLOT ? 0 : sl + SLOT;
  }
  mm(B, 3);
  TS(3);
  {
    f32x2 st = {s0 + s1, q0 + q1};
    *(LAS f32x2*)(size_t)(L_ST + TYPE * 2048 + (64 * m + 32 * n + r) * 16 + h * 8) = st;
    if (h == 0) {
      *(LAS float*)(size_t)(L_CS + id0 * 256 + m * 128 + r * 4) = cs0[0];
      *(LAS float*)(size_t)(L_CS + id1 * 256 + m * 128 + r * 4) = cs1[0];
      if (EX && n == 0) *(LAS float*)(size_t)(L_CS + 8 * 256 + m * 128 + r * 4) = csx[0];
    }
  }
  bar();
  TS(4);
  const u64 tag = get_tag();
  TokC tc[2]; u32x4 mf[2];
#pragma unroll
  for (int b = 0; b < 2; ++b) {
    const unsigned t = 64 * m + 32 * (n ^ b) + r;
    const f32x4 u = lds128f(L_ST + t * 16), v = lds128f(L_ST + 2048 + t * 16);
    const float mean = ((u[0] + u[2]) + (v[0] + v[2])) * (1.f / DM);
    const float var = __builtin_fmaxf(((u[1] + u[3]) + (v[1] + v[3])) * (1.f / DM) - mean * mean, 0.f);
    const float den = rbf(__builtin_sqrtf(rbf(rbf(var) + 1e-5f)));
    const float rs = 1.f / den;
    const unsigned nm = pk2(-mean, -mean);
    mf[b] = u32x4{nm, nm, nm, nm};
    tc[b].rs = rs; tc[b].kk = den * den;
    tc[b].c0 = -2.302208198f * rs;
    tc[b].c1 = -0.1029432397f * rs * rs * rs;
  }
  auto csfrag = [&](int id) {
    const float c = *(const LAS float*)(size_t)(L_CS + id * 256 + r * 4) + *(const LAS float*)(size_t)(L_CS + id * 256 + 128 + r * 4);
    const float c1 = rbf(c), c2 = rbf(c - c1), c3 = (c - c1) - c2;
    return u32x4{h ? 0u : pk2(c1, c2), h ? 0u : pk2(c3, 0.f), 0u, 0u};
  };
  {
    const u32x4 f0 = csfrag(id0), f1 = csfrag(id1);
    if constexpr (TOKROW) {
      acc00 = mfma32(mf[0], f0, acc00); acc01 = mfma32(mf[1], f0, acc01);
      acc10 = mfma32(mf[0], f1, acc10); acc11 = mfma32(mf[1], f1, acc11);
    } else {
      acc10 = mfma32(f1, mf[0], acc10); acc11 = mfma32(f1, mf[1], acc11);
      acc00 = mfma32(f0, mf[0], acc00); acc01 = mfma32(f0, mf[1], acc01);
      if constexpr (EX) accx = mfma32(csfrag(8), mf[0], accx);
    }
  }
  char* ws = (char*)a.ws;
  const __amdgpu_buffer_rsrc_t rw = __builtin_amdgcn_make_buffer_rsrc(a.ws, 0, WS_END, 0x00020000);
  const unsigned av = (r >> 4) * (NSTEP * 1024) + h * 256 + (r & 15) * 16;
  const int t0 = g.rt * 4 + 2 * m + n, t1 = g.rt * 4 + 2 * m + (n ^ 1);
  const unsigned fh = WS_FH + (g.rt * 16 + ROLE * 8 + g.g8) * 128;
  if constexpr (TYPE == 1) {
    const int u = g.u0 + (ROLE ? 2 : 0) + 2 * n;
    store_act(rw, av, t0, u + 1, geglu(acc10, tc[0]));
    store_act(rw, av, t1, u + 1, geglu(acc11, tc[1]));
    store_act(rw, av, t0, u, geglu(acc00, tc[0]));
    store_act(rw, av, t1, u, geglu(acc01, tc[1]));
    TS(5);
    publish(C_H, ROLE ? 8 : 4, lane, ws, fh, tag);
    TS(6);
    if constexpr (ROLE == 1) {
      poll(ws, WS_FK + (g.g8 * 16 + (lane & 15)) * 128, tag);
      TS(7);
      u32x4 pv[16];
#pragma unroll
      for (int i = 0; i < 16; ++i)
        pv[i] = __builtin_amdgcn_raw_buffer_load_b128(rw, lane * 16, WS_KP + (g.g8 * 16 + i) * 4096 + n * 2048 + m * 1024, 16);
      const u32x4 zv = __builtin_amdgcn_raw_buffer_load_b128(rw, (lane >> 2) * 256 + (lane & 3) * 32, WS_ZP + g.g8 * 4096 + n * 128 + m * 16, 16);
      f32x4 sum = __builtin_bit_cast(f32x4, pv[0]);
#pragma unroll
      for (int i = 1; i < 16; ++i) sum += __builtin_bit_cast(f32x4, pv[i]);
      *(LAS u32x2*)(size_t)(L_KX + n * 2048 + lane * 16 + m * 8) = u32x2{pk2(sum[0], sum[1]), pk2(sum[2], sum[3])};
      f32x4 z = __builtin_bit_cast(f32x4, zv);
#pragma unroll
      for (int o = 4; o < 64; o <<= 1) {
#pragma unroll
        for (int i = 0; i < 4; ++i) z[i] += __shfl_xor(z[i], o);
      }
      if (lane < 4) *(LAS u32x2*)(size_t)(L_KX + n * 2048 + 1024 + lane * 16 + m * 8) = u32x2{pk2(z[0], z[1]), pk2(z[2], z[3])};
      lds_inc(C_KVR0 + n, lane);
      TS(8);
    }
  } else if constexpr (ROLE == 1) {
    store_act(rw, av, t0, g.u0 + n, geglu(acc10, tc[0]));
    store_act(rw, av, t1, g.u0 + n, geglu(acc11, tc[1]));
    store_act(rw, av, t0, g.u0 + 6, geglu(accx, tc[0]));
    TS(5);
    u32x4 pq[2][2];
#pragma unroll
    for (int b = 0; b < 2; ++b) {
      const f32x16& c = b ? acc01 : acc00;
#pragma unroll
      for (int i = 0; i < 8; ++i) pq[b][i >> 2][i & 3] = pk2(phi(c[2 * i] * tc[b].rs), phi(c[2 * i + 1] * tc[b].rs));
    }
    publish(C_H, 8, lane, ws, fh, tag);
    TS(6);
    lds_wait(C_KVR0 + n, 2);
    TS(7);
    const u32x4 kvf = lds128(L_KX + n * 2048 + lane * 16), zf = lds128(L_KX + n * 2048 + 1024 + (blk * 2 + h) * 16);
    const u32x4 zero = {0, 0, 0, 0};
    const u32x4 kv0 = blk ? zero : kvf, kv1 = blk ? kvf : zero, z0 = blk ? zero : zf, z1 = blk ? zf : zero;
#pragma unroll
    for (int b = 0; b < 2; ++b) {
      f32x16 num = {}, den = {};
      num = mfma32(kv0, pq[b][0], num); den = mfma32(z0, pq[b][0], den);
      num = mfma32(kv1, pq[b][1], num); den = mfma32(z1, pq[b][1], den);
#pragma unroll
      for (int s = 0; s < 2; ++s) {
        const float rd = 1.f / (den[8 * s] + 1e-6f);
        u32x4 o;
#pragma unroll
        for (int i = 0; i < 4; ++i) o[i] = pk2(num[8 * s + 2 * i] * rd, num[8 * s + 2 * i + 1] * rd);
        store_act(rw, av, b ? t1 : t0, 86 + g.g8 * 4 + 2 * n + s, o);
      }
    }
    publish(C_O, 4, lane, ws, WS_FO + (g.rt * 8 + g.g8) * 128, tag);
    TS(8);
  } else {
    if (h == 0) {
      *(LAS float*)(size_t)(L_RS + w * 256 + r * 4) = tc[0].rs;
      *(LAS float*)(size_t)(L_RS + w * 256 + 128 + r * 4) = tc[1].rs;
    }
    CFENCE();
    f32x16 kv = {}, zz = {};
    const u32x4 ones = {0x3f803f80u, 0x3f803f80u, 0x3f803f80u, 0x3f803f80u};
#pragma unroll
    for (int b = 0; b < 2; ++b) {
      const f32x16& ck = b ? acc01 : acc00;
      const f32x16& cv = b ? acc11 : acc10;
#pragma unroll
      for (int s = 0; s < 2; ++s) {
        u32x4 pk, pv;
#pragma unroll
        for (int j = 0; j < 2; ++j) {
          const f32x4 rs = lds128f(L_RS + w * 256 + b * 128 + (16 * s + 8 * j + 4 * h) * 4);
          const int i = 8 * s + 4 * j;
          pk[2 * j] = pk2(phi(ck[i] * rs[0]), phi(ck[i + 1] * rs[1]));
          pk[2 * j + 1] = pk2(phi(ck[i + 2] * rs[2]), phi(ck[i + 3] * rs[3]));
          pv[2 * j] = pk2(cv[i] * rs[0], cv[i + 1] * rs[1]);
          pv[2 * j + 1] = pk2(cv[i + 2] * rs[2], cv[i + 3] * rs[3]);
        }
        kv = mfma32(pk, pv, kv); zz = mfma32(pk, ones, zz);
      }
    }
    f32x4 o[4];
#pragma unroll
    for (int i = 0; i < 4; ++i) {
      o[0][i] = blk ? kv[8 + i] : kv[i]; o[1][i] = blk ? kv[12 + i] : kv[4 + i];
      o[2][i] = blk ? zz[8 + i] : zz[i]; o[3][i] = blk ? zz[12 + i] : zz[4 + i];
    }
    TS(5);
    if (m == 1) {
#pragma unroll
      for (int i = 0; i < 4; ++i) *(LAS f32x4*)(size_t)(L_KX + n * 4096 + i * 1024 + lane * 16) = o[i];
      lds_inc(C_KVX0 + n, lane);
    } else {
      lds_wait(C_KVX0 + n, 1);
#pragma unroll
      for (int i = 0; i < 4; ++i) o[i] += lds128f(L_KX + n * 4096 + i * 1024 + lane * 16);
      const unsigned kb = (g.g8 * 16 + g.rt);
      st128(rw, lane * 16 + (WS_KP + kb * 4096 + n * 2048), __builtin_bit_cast(u32x4, o[0]));
      st128(rw, lane * 16 + (WS_KP + kb * 4096 + n * 2048 + 1024), __builtin_bit_cast(u32x4, o[1]));
      if ((lane & 15) == 0) {
        st128(rw, (blk * 2 + h) * 32 + (WS_ZP + kb * 256 + n * 128), __builtin_bit_cast(u32x4, o[2]));
        st128(rw, (blk * 2 + h) * 32 + 16 + (WS_ZP + kb * 256 + n * 128), __builtin_bit_cast(u32x4, o[3]));
      }
      publish(C_K, 2, lane, ws, WS_FK + kb * 128, tag);
    }
    TS(6);
  }
}

DI void phase2(const Args& a, const Geo& g, int w, int lane TS_ARG) {
  const int tb = w & 3, kh = w >> 2;
  const unsigned gq = lane >> 4, q = (lane >> 2) & 3, p = lane & 3, tok = lane & 15;
  const __amdgpu_buffer_rsrc_t rw = __builtin_amdgcn_make_buffer_rsrc(a.ws, 0, WS_END, 0x00020000);
  const unsigned abase = WS_ACT + ((g.tb64 * 4 + tb) * NSTEP + kh) * 1024;
  const unsigned row = 16 * (gq >> 1) + 4 * (gq & 1) + q, sw = (row >> 1) & 3;
  unsigned wa[4];
#pragma unroll
  for (int nb = 0; nb < 4; ++nb) wa[nb] = kh * 4096 + row * 128 + ((nb ^ sw) * 32) + p * 8;
  const unsigned xoff = (g.tb64 * 64 + tb * 16 + tok) * 1024 + g.nt * 128 + kh * 64 + gq * 8;
  const u32x2 xr0 = *(const GAS u32x2*)((const char*)a.x + xoff), xr1 = *(const GAS u32x2*)((const char*)a.x + xoff + 32);

  constexpr int PW = 6;
  u32x4 act[30];
  auto ldact = [&](int i) {
    const int j = i < 22 ? 2 * i : 2 * i - 1;
    act[i] = __builtin_amdgcn_raw_buffer_load_b128(rw, lane * 16, abase + j * 1024, 16);
  };
  f32x4 f[4] = {}, s[4] = {};
  auto step = [&](int i) {
    const int reg = i < 17 ? 2 * i : (i < 22 ? 2 * i - 34 : 2 * i - 35);
    u32x4 wf[4];
#pragma unroll
    for (int nb = 0; nb < 4; ++nb) wf[nb] = ldsfrag(wa[nb] + reg * 4096, wa[nb] + reg * 4096 + 1024);
#pragma unroll
    for (int nb = 0; nb < 4; ++nb) { if (i < 22) f[nb] = mfma16(wf[nb], act[i], f[nb]); else s[nb] = mfma16(wf[nb], act[i], s[nb]); }
  };
  TS(9);
  lds_wait(C_RH1, 1);
  TS(10);
#pragma unroll
  for (int i = 0; i < PW; ++i) ldact(i);
  lds_wait(C_W0, 4);
  SB();
#pragma unroll
  for (int i = 0; i < 30; ++i) {
    if (i + PW == 8) { lds_wait(C_RH2, 1); TS(11); }
    if (i + PW == 21) { lds_wait(C_RO, 1); TS(12); }
    if (i == 17) lds_wait(C_W1, 3);
    if (i == 23) lds_wait(C_W2, 3);
    if (i + PW < 30) ldact(i + PW);
    SB();
    if (i == 21) { if (kh == 0) step(i); }
    else step(i);
    SB();
    if (i == 5) lds_inc(C_F1, lane);
    if (i == 12) lds_inc(C_F2, lane);
    if (i == 16) lds_inc(C_F3, lane);
    if (i == 21) {
      *(LAS f32x4*)(size_t)(L_FX + w * 2048 + lane * 16) = kh ? f[0] : f[2];
      *(LAS f32x4*)(size_t)(L_FX + w * 2048 + 1024 + lane * 16) = kh ? f[1] : f[3];
    }
  }
  TS(13);
  lds_wait(C_F3, 8);
  *(LAS f32x4*)(size_t)(L_SX + w * 2048 + lane * 16) = kh ? s[0] : s[2];
  *(LAS f32x4*)(size_t)(L_SX + w * 2048 + 1024 + lane * 16) = kh ? s[1] : s[3];
  lds_inc(C_P0 + w, lane);
  lds_wait(C_P0 + (w ^ 4), 1);
#pragma unroll
  for (int b = 0; b < 2; ++b) {
    const f32x4 ff = (kh ? f[2 + b] : f[b]) + lds128f(L_FX + (w ^ 4) * 2048 + b * 1024 + lane * 16);
    const f32x4 ss = (kh ? s[2 + b] : s[b]) + lds128f(L_SX + (w ^ 4) * 2048 + b * 1024 + lane * 16);
    const u32x2 xr = b ? xr1 : xr0;
    float o[4];
#pragma unroll
    for (int i = 0; i < 4; ++i) {
      const float sg = ff[i] * __builtin_amdgcn_rcpf(1.f + __builtin_amdgcn_exp2f(ff[i] * -1.44269504f));
      o[i] = __builtin_fmaf(ss[i], sg, (i & 1) ? bfhi(xr[i >> 1]) : bflo(xr[i >> 1]));
    }
    *(GAS u32x2*)((char*)a.out + xoff + b * 32) = u32x2{pk2(o[0], o[1]), pk2(o[2], o[3])};
  }
  TS(14);
}

__global__ __launch_bounds__(768, 3) void fused_block(Args a) {
  __shared__ __attribute__((aligned(16))) char smem[LDS_BYTES];
  TS_DECL;
  const int lane = threadIdx.x & 63, w = __builtin_amdgcn_readfirstlane(threadIdx.x >> 6);
  asm volatile("" ::"v"((unsigned)(size_t)(LAS char*)smem) : "memory");
  const int j = blockIdx.x & 7, i = blockIdx.x >> 3, cl = i >> 3, role = cl & 1;
  Geo g;
  g.rt = 8 * (j >> 2) + (i & 7);
  g.g8 = 2 * (j & 3) + (cl >> 1);
  g.u0 = role ? (g.g8 == 7 ? 79 : 32 + 7 * g.g8) : 4 * g.g8;
  g.rot = ROT_MODE == 1 ? 0 : ROT_MODE == 2 ? 4 * ((i + cl) & 1) : ROT_MODE == 4 ? 2 * ((i + cl) & 3) : ((i + 2 * cl) & 7);
  g.tb64 = 4 * j + (i & 3);
  g.nt = i >> 2;
  if (w >= 8) {
    __builtin_amdgcn_s_setprio(3);
    if (role) loader<1>(a, g, w - 8, lane TS_PASS); else loader<0>(a, g, w - 8, lane TS_PASS);
  } else {
    if (w == 0) {
      const u64 e = __hip_atomic_load((GAS u64*)((char*)a.ws + WS_EPOCH), __ATOMIC_RELAXED, __HIP_MEMORY_SCOPE_AGENT);
      if (lane < C_N) *ctl(lane) = 0;
      if (lane == 0) *(LAS u64*)(size_t)(L_CTL + 4 * C_TAG) = e + 0x9E3779B97F4A7C15ull;
    }
    if (role) { if (w < 4) phase1<1, 0>(a, g, w, lane TS_PASS); else phase1<1, 1>(a, g, w, lane TS_PASS); }
    else { if (w < 4) phase1<0, 0>(a, g, w, lane TS_PASS); else phase1<0, 1>(a, g, w, lane TS_PASS); }
    phase2(a, g, w, lane TS_PASS);
  }
}

extern "C" void kernel_launch(void* const* d_in, const int* in_sizes, int n_in, void* d_out, int out_size, void* d_ws,
                              size_t ws_size, hipStream_t stream) {
  Args a;
  a.x = d_in[0]; a.wqkv = d_in[1]; a.wo = d_in[2]; a.wg = d_in[4]; a.wu = d_in[5]; a.wd = d_in[6];
  a.out = d_out; a.ws = d_ws;
  fused_block<<<256, 768, 0, stream>>>(a);
}
```

```cpp
#include <hip/hip_runtime.h>

typedef unsigned long long u64;
typedef __attribute__((ext_vector_type(8))) __bf16 bf16x8;
typedef __attribute__((ext_vector_type(2))) __bf16 bf16x2;
typedef __attribute__((ext_vector_type(2))) float f32x2;
typedef __attribute__((ext_vector_type(4))) float f32x4;
typedef __attribute__((ext_vector_type(2))) unsigned u32x2;
typedef __attribute__((ext_vector_type(4))) unsigned u32x4;
typedef __attribute__((ext_vector_type(4))) int i32x4;
typedef __attribute__((ext_vector_type(8))) u64 u64x8;
typedef __attribute__((ext_vector_type(4))) short s16x4;
#define DI __device__ __forceinline__
#define LAS __attribute__((address_space(3)))
#define GAS __attribute__((address_space(1)))
#define SB() __builtin_amdgcn_sched_barrier(0)
#define CFENCE() asm volatile("" ::: "memory")
#define UNROLL _Pragma("unroll")
#define NOUNROLL _Pragma("unroll 1")

#define ROT_MODE 2
#define OUT_AUX 16
#define GATHER_AT 4
#define POLL_SLEEP 2
#define PAD_Q 2
#define PAD_KV 1
#define PAD_GU 3
#define PAD_W 4
#define PAD_E -1
#define HOLD 3
#define SIDE_D0 0
#define SIDE_D1 0
#define TOUCH_AT 6
#define P2_PW 5
#define PARKW_PRIO 0
#define PREWARM 1
#define GATHER_AT1 4
#define GATHER_PRIO 3
#define P2MAP 0
#define PFD 3
#define PFK 5
#define PARK0 36
#define PARKF 4
#define FIRST_DRAIN 1
#define KV_AT 10
#define LOADER_PRIO 2
#define XP 0
#define CODE_LINES 470

constexpr int SEQ = 2048, DM = 512, DF = 1376;
constexpr int NSTEP = 59;
constexpr unsigned WS_EPOCH = 0;
constexpr unsigned WS_FA = 4096;
constexpr unsigned WS_FK = WS_FA + 256 * 256;
constexpr unsigned WS_FC = WS_FK + 128 * 128;
constexpr unsigned WS_FT = WS_FC + 256 * 128;
constexpr unsigned WS_FQ = WS_FT + 256 * 128;
constexpr unsigned WS_KP = 262144;
constexpr unsigned WS_ZP = WS_KP + 8 * 16 * 4096;
constexpr unsigned WS_ACT = 1048576;
constexpr unsigned WS_CSP = 10u << 20;
constexpr unsigned WS_CQP = WS_CSP + 256 * 1024;
constexpr unsigned WS_STP = WS_CQP + 128 * 128;
constexpr unsigned WS_END = WS_STP + SEQ * 16;
constexpr unsigned WS_DBG = 16u << 20;
constexpr unsigned L_A = 49152, L_G = 49152, L_U = 104448;
constexpr unsigned L_FX = 136 * 1024;
constexpr unsigned L_SX = 26 * 4096;
constexpr unsigned L_KX = 32 * 1024;
constexpr unsigned L_ZX = 144 * 1024;
constexpr unsigned L_KF = 145 * 1024;
constexpr unsigned L_TC = 152 * 1024;
constexpr unsigned L_CS = 154 * 1024;
constexpr unsigned L_FREE = 156 * 1024;
constexpr unsigned L_CTL = L_FREE + 1024;
constexpr unsigned L_LND = L_CTL + 256;
constexpr unsigned L_NM = L_FREE + 2048;
constexpr unsigned L_RS = L_FREE + 2560;
constexpr unsigned L_DUMP = L_FREE + 3072;
enum { C_A0 = 0  , C_K = C_A0 + 4, C_X, C_KF, C_LVL, C_W1, C_W2, C_P0  , C_TAG = 32 };
enum { F_FULL = 0  , F_RDY = 12, F_W0, F_WB };
enum { M_PRE = 17, M_MID, M_FX };
constexpr unsigned LDS_BYTES = 163840;
constexpr float GELU_C0 = -2.302208198f;
constexpr u64 TAG_STEP = 0x9E3779B97F4A7C15ull;
constexpr unsigned SPIN_CAP = 1u << 20;

struct Args {
  const void* x; const void* wqkv; const void* wo; const void* wg; const void* wu; const void* wd; void* out; void* ws;
};

DI bf16x8 asbf(u32x4 v) { return __builtin_bit_cast(bf16x8, v); }
DI f32x4 mfma(u32x4 a, u32x4 b, f32x4 c) { return __builtin_amdgcn_mfma_f32_16x16x32_bf16(asbf(a), asbf(b), c, 0, 0, 0); }
DI void mfma_acc(f32x4& c, u32x4 a, u32x4 b) { asm volatile("v_mfma_f32_16x16x32_bf16 %0, %1, %2, %0" : "+v"(c) : "v"(a), "v"(b)); }
DI void mfma_new(f32x4& c, u32x4 a, u32x4 b) { asm volatile("v_mfma_f32_16x16x32_bf16 %0, %1, %2, 0" : "=&v"(c) : "v"(a), "v"(b)); }
template <bool FIRST> DI void mfma_k(f32x4& c, u32x4 a, u32x4 b) { if constexpr (FIRST) mfma_new(c, a, b); else mfma_acc(c, a, b); }
DI void mfma_done() { SB(); asm volatile("s_nop 7\n\ts_nop 1" ::: "memory"); SB(); }
DI f32x4 mfma_k16(u32x2 a, u32x2 b, f32x4 c) {
  return __builtin_amdgcn_mfma_f32_16x16x16bf16_1k(__builtin_bit_cast(s16x4, a), __builtin_bit_cast(s16x4, b), c, 0, 0, 0);
}
DI unsigned pk2(f32x2 v) { return __builtin_bit_cast(unsigned, __builtin_convertvector(v, bf16x2)); }
DI unsigned pk2(float a, float b) { return pk2(f32x2{a, b}); }
DI float rbf(float a) { return __uint_as_float(pk2(a, a) << 16); }
DI float bflo(unsigned w) { return __uint_as_float(w << 16); }
DI float bfhi(unsigned w) { return __uint_as_float(w & 0xffff0000u); }
DI f32x2 lo2(f32x4 v) { return f32x2{v[0], v[1]}; }
DI f32x2 hi2(f32x4 v) { return f32x2{v[2], v[3]}; }
DI f32x2 sp(float a) { return f32x2{a, a}; }
DI f32x2 fma2(f32x2 a, f32x2 b, f32x2 c) { return __builtin_elementwise_fma(a, b, c); }
DI float opq(float a) { asm("" : "+v"(a)); return a; }
DI f32x4 add4(f32x4 a, f32x4 b) { return f32x4{opq(a[0] + b[0]), opq(a[1] + b[1]), opq(a[2] + b[2]), opq(a[3] + b[3])}; }
DI u32x4 lds128(unsigned a) { return *(const LAS u32x4*)(size_t)a; }
DI f32x4 lds128f(unsigned a) { return *(const LAS f32x4*)(size_t)a; }
DI u32x2 ldstr(unsigned a) { return __builtin_bit_cast(u32x2, __builtin_amdgcn_ds_read_tr16_b64_v4i16((LAS s16x4*)(size_t)a)); }
DI u32x4 cat(u32x2 l, u32x2 h) { return u32x4{l[0], l[1], h[0], h[1]}; }
DI i32x4 mksrd(const void* p, unsigned bytes) {
  u64 a = (u64)p; return i32x4{(int)(unsigned)a, (int)((unsigned)(a >> 32) & 0xffffu), (int)bytes, 0x00020000};
}
DI void dma(unsigned lds, unsigned voff, i32x4 srd, unsigned soff) {
  asm volatile("s_mov_b32 m0, %0\n\ts_nop 0\n\tbuffer_load_dwordx4 %1, %2, %3 offen lds" ::"s"(lds), "v"(voff), "s"(srd), "s"(soff) : "memory");
}
DI void touch(const void* p) {
  asm volatile("s_mov_b32 m0, %0\n\ts_nop 0\n\tglobal_load_lds_dword %1, off" ::"s"(L_DUMP), "v"(p) : "memory");
}
template <int N> DI void wait_vm() { asm volatile("s_waitcnt vmcnt(%0)" ::"i"(N) : "memory"); }
DI LAS unsigned* ctl(int i) { return (LAS unsigned*)(size_t)(L_CTL + 4 * i); }
DI u64 get_tag() { return *(const LAS u64*)(size_t)(L_CTL + 4 * C_TAG); }
DI unsigned peek(int i) { CFENCE(); unsigned v = __hip_atomic_load(ctl(i), __ATOMIC_RELAXED, __HIP_MEMORY_SCOPE_WORKGROUP); CFENCE(); return v; }
DI void spin_lds(unsigned addr, unsigned want) {
  unsigned v, n;
  asm volatile("s_mov_b32 %1, %4\n"
               "1:\n\t"
               "ds_read_b32 %0, %2\n\t"
               "s_sub_u32 %1, %1, 1\n\t"
               "s_waitcnt lgkmcnt(0)\n\t"
               "v_cmp_gt_u32 vcc, %3, %0\n\t"
               "s_cbranch_scc1 2f\n\t"
               "s_cbranch_vccnz 1b\n"
               "2:"
               : "=&v"(v), "=&s"(n) : "v"(addr), "s"(want), "i"(SPIN_CAP) : "memory", "vcc", "scc");
}
DI void lds_wait(int i, unsigned want) { spin_lds(L_CTL + 4 * i, want); }
DI void wait_free(unsigned want, int lane) { spin_lds(L_FREE + (lane & 3) * 256, want); }
DI void ensure(unsigned v, int i, unsigned want) {
  if (__builtin_expect((unsigned)__builtin_amdgcn_readfirstlane(v) < want, 0)) lds_wait(i, want);
  CFENCE();
}
DI void landed(int i, int mb, const void* nz) {
  asm volatile("s_mov_b32 m0, %0\n\ts_mov_b64 exec, 1\n\tglobal_load_lds_dword %1, %2\n\ts_mov_b64 exec, -1"
               ::"s"(L_LND + 8 * i + 4 * mb), "v"(0u), "s"(nz) : "memory");
}
DI unsigned both_addr(int lane) { return L_LND + (lane & 1) * 4; }
DI unsigned peek2(unsigned ba, int i) { CFENCE(); unsigned v = *(volatile LAS unsigned*)(size_t)(ba + 8 * i); CFENCE(); return v; }
DI void ensure2(unsigned v, unsigned ba, int i) {
  if (__builtin_expect(__builtin_amdgcn_ballot_w64(v == 0) != 0, 0)) spin_lds(ba + 8 * i, 1);
  CFENCE();
}
DI void lds_inc(int i) {
  asm volatile("s_mov_b64 exec, 1\n\tds_add_u32 %0, %1\n\ts_mov_b64 exec, -1" ::"v"(L_CTL + 4 * i), "v"(1u) : "memory");
}
DI void lds_set(int i) { CFENCE(); *(volatile LAS unsigned*)ctl(i) = 1u; CFENCE(); }
template <int N> DI void set_flags(char* ws, unsigned off, u64 tag) {
  asm volatile("s_mov_b64 exec, %3\n\tglobal_store_dwordx2 %0, %1, %2 sc1\n\ts_mov_b64 exec, -1"
               ::"v"(off), "v"(tag), "s"(ws), "s"((1ull << N) - 1) : "memory");
}
template <int N, bool DRAIN = true> DI void publish(int i, char* ws, unsigned off, u64 tag) {
  unsigned old, so;
  if constexpr (DRAIN) wait_vm<0>();
  asm volatile("s_mov_b64 exec, 1\n\t"
               "ds_add_rtn_u32 %0, %2, %3\n\t"
               "s_waitcnt lgkmcnt(0)\n\t"
               "v_readfirstlane_b32 %1, %0\n\t"
               "s_cmp_eq_u32 %1, %7\n\t"
               "s_cselect_b32 exec_lo, 0xffff, 0\n\t"
               "global_store_dwordx2 %4, %5, %6 sc1\n\t"
               "s_mov_b64 exec, -1"
               : "=&v"(old), "=&s"(so) : "v"(L_CTL + 4 * i), "v"(1u), "v"(off), "v"(tag), "s"(ws), "i"(N - 1) : "memory", "scc");
}
template <int N> DI void poll(char* ws, unsigned off, u64 tag) {
  u64 v; unsigned n;
  asm volatile("s_mov_b32 %1, %6\n\t"
               "s_mov_b64 exec, %8\n"
               "1:\n\t"
               "global_atomic_add_x2 %0, %2, %5, %3 sc0 sc1\n\t"
               "s_waitcnt vmcnt(0)\n\t"
               "v_cmp_ne_u64 vcc, %4, %0\n\t"
               "s_cbranch_vccz 2f\n\t"
               "s_sleep %7\n\t"
               "s_sub_u32 %1, %1, 1\n\t"
               "s_cbranch_scc0 1b\n"
               "2:\n\t"
               "s_mov_b64 exec, -1"
               : "=&v"(v), "=&s"(n)
               : "v"(off), "s"(ws), "v"(tag), "v"(0ull), "i"(SPIN_CAP), "i"(POLL_SLEEP), "s"(N == 64 ? ~0ull : (1ull << (N & 63)) - 1)
               : "memory", "vcc", "scc");
}
DI void poll_levels(char* ws, unsigned off, u64 tag) {
  u64 v; unsigned n, l, vl;
  asm volatile("s_mov_b32 %1, %8\n\t"
               "s_mov_b32 exec_hi, 0\n"
               "1:\n\t"
               "global_atomic_add_x2 %0, %4, %7, %5 sc0 sc1\n\t"
               "s_waitcnt vmcnt(0)\n\t"
               "v_cmp_ne_u64 vcc, %6, %0\n\t"
               "s_ff1_i32_b32 %2, vcc_lo\n\t"
               "s_min_u32 %2, %2, 32\n\t"
               "s_lshr_b32 %2, %2, 3\n\t"
               "v_mov_b32 %3, %2\n\t"
               "ds_write_b32 %10, %3\n\t"
               "s_cbranch_vccz 2f\n\t"
               "s_sleep %9\n\t"
               "s_sub_u32 %1, %1, 1\n\t"
               "s_cbranch_scc0 1b\n"
               "2:\n\t"
               "s_mov_b64 exec, -1"
               : "=&v"(v), "=&s"(n), "=&s"(l), "=&v"(vl)
               : "v"(off), "s"(ws), "v"(tag), "v"(0ull), "i"(SPIN_CAP), "i"(POLL_SLEEP), "v"(L_CTL + 4 * C_LVL)
               : "memory", "vcc", "scc");
}
DI void st128(__amdgpu_buffer_rsrc_t rw, unsigned off, u32x4 v) { __builtin_amdgcn_raw_buffer_store_b128(v, rw, off, 0, 16); }
DI void st64(__amdgpu_buffer_rsrc_t rw, unsigned off, u32x2 v) { __builtin_amdgcn_raw_buffer_store_b64(v, rw, off, 0, 16); }

#define TS(i)
#define TSD(i)
#define TS_DECL
#define TS_ARG
#define TS_PASS

DI int fresh_lane() { int l; asm volatile("v_mbcnt_lo_u32_b32 %0, -1, 0\n\tv_mbcnt_hi_u32_b32 %0, -1, %0" : "=v"(l)); return l; }

struct Geo { int rt, ct, g8, u0, rot, tb64, nt; };

#define DMA1(V, STEP) "buffer_load_dwordx4 " V ", %2, %0 offen lds\n\t.if %c6 >= 0\n\ts_nop %c6\n\t.endif\n\ts_add_u32 m0, m0, " STEP "\n\t"
#define DMAS(V) DMA1(V, "0x400") "s_add_u32 %0, %0, %5\n\t"
#define DMA_ARGS : "+s"(so) : "s"(lds), "s"(srd), "v"(v0), "v"(v1), "s"(inc), "i"(PAD) : "memory", "scc"
template <int PAD> DI void dma8(unsigned lds, i32x4 srd, unsigned so, unsigned inc, unsigned v0, unsigned v1) {
  asm volatile("s_mov_b32 m0, %1\n\ts_nop 0\n\t" DMAS("%3") DMAS("%4") DMAS("%3") DMAS("%4") DMAS("%3") DMAS("%4") DMAS("%3") DMAS("%4") DMA_ARGS);
}
template <int PAD> DI void dma4(unsigned lds, i32x4 srd, unsigned so, unsigned inc, unsigned v0, unsigned v1) {
  asm volatile("s_mov_b32 m0, %1\n\ts_nop 0\n\t" DMAS("%3") DMAS("%4") DMAS("%3") DMAS("%4") DMA_ARGS);
}
template <int PAD> DI void dma2(unsigned lds, i32x4 srd, unsigned so, unsigned inc, unsigned v0) {
  const unsigned v1 = v0;
  asm volatile("s_mov_b32 m0, %1\n\ts_nop 0\n\t" DMAS("%3") "buffer_load_dwordx4 %4, %2, %0 offen lds" DMA_ARGS);
}
#define DMAN(V) "buffer_load_dwordx4 " V ", %2, %0 offen lds\n\ts_add_u32 m0, m0, 0x400\n\ts_nop %c12\n\t"
template <int PAD> DI void dma9(unsigned lds, i32x4 srd, unsigned so, const unsigned (&v)[9]) {
  asm volatile("s_mov_b32 m0, %1\n\ts_nop 0\n\t" DMAN("%3") DMAN("%4") DMAN("%5") DMAN("%6") DMAN("%7") DMAN("%8") DMAN("%9") DMAN("%10") DMAN("%11")
               :: "s"(so), "s"(lds), "s"(srd), "v"(v[0]), "v"(v[1]), "v"(v[2]), "v"(v[3]), "v"(v[4]), "v"(v[5]), "v"(v[6]), "v"(v[7]), "v"(v[8]),
                  "i"(PAD + 1) : "memory", "scc");
}
constexpr int fpair(int g8) { return g8 == 7 ? 39 : (16 + 9 * g8 + (g8 & 1)) / 2; }
constexpr int w2row(int j) {
  return 32 * (j < 32 ? fpair((j & 15) >> 1) + 2 * (j & 1) + (j >> 4) : j == 32 ? 12 : j == 33 ? 21 : j < 42 ? j - 34 : j == 42 ? 30 : j - 43);
}
template <int J0, int J1, int PAD> DI void w2fill(unsigned lb, unsigned vw, i32x4 sd, unsigned sb) {
  if constexpr (J0 < J1) { dma2<PAD>(lb + J0 * 4096, sd, sb + w2row(J0) * 1024, 8192, vw); w2fill<J0 + 1, J1, PAD>(lb, vw, sd, sb); }
}

struct Ld { i32x4 sx, sa, sg; const void* nz; unsigned vx0, vx1, va0, va1, xo, ao, go; };
template <int ROLE> DI void loader(const Args& a, const Geo& g, const Ld& L, int mb TS_ARG) {
  const int lane = fresh_lane();
  const unsigned lg = mb ? L_U : L_G;
  unsigned vg[9];
  if constexpr (ROLE == 1) {
    UNROLL for (int i = 0; i < 9; ++i) {
      const unsigned idx = 64 * i + lane, rho = __umul24(idx, 3641) >> 16;
      unsigned cc = idx + 2 * (g.g8 & 1) - __umul24(rho, 18); cc = min(cc, cc - 18);
      vg[i] = __umul24(rho ^ ((rho & 8) >> 1), 2752) + cc * 16;
      dma(lg + i * 1024, vg[i], L.sg, L.go + g.rot * (64 * 2752));
      SB();
    }
    dma9<-1>(lg + 9216, L.sg, L.go + (g.rot * 64 + 32) * 2752, vg);
    landed(F_FULL, mb, L.nz);
    TSD(1);
  }
  const unsigned r8 = lane >> 3, c8 = lane & 7;
  const unsigned vw = r8 * 1024 + (c8 ^ (r8 & 6)) * 16;
  const unsigned fa = L_FREE + (lane & 3) * 256;
  constexpr int T0 = ROLE ? 1 : 2, PAD = ROLE ? PAD_Q : PAD_KV;
  unsigned slot = T0, k0 = ((g.rot + T0) & 7) * 64, mark = 0;
  NOUNROLL for (int t = T0; t < 8; ++t) {
    if (__builtin_expect(__any((int)mark < t - 2), 0)) spin_lds(fa, t - 2);
    TSD(2 * t);
    if constexpr (ROLE == 0) {
      dma4<PAD>(slot * 16384 + mb * 8192, L.sx, L.xo + k0 * 2, 8192, L.vx0, L.vx1);
      dma4<PAD>(L_A + mb * 8192 + slot * 16384, L.sa, L.ao + k0 * 3072, 8 * 3072, L.va0, L.va1);
      dma4<PAD>(slot * 16384 + mb * 8192 + 4096, L.sx, L.xo + k0 * 2 + 32768, 8192, L.vx0, L.vx1);
      dma4<PAD>(L_A + mb * 8192 + slot * 16384 + 4096, L.sa, L.ao + (k0 + 32) * 3072, 8 * 3072, L.va0, L.va1);
    } else {
      dma4<PAD>(slot * 16384 + mb * 8192, L.sx, L.xo + k0 * 2, 8192, L.vx0, L.vx1);
      dma9<PAD>(lg + slot * 18432, L.sg, L.go + k0 * 2752, vg);
      dma4<PAD>(slot * 16384 + mb * 8192 + 4096, L.sx, L.xo + k0 * 2 + 32768, 8192, L.vx0, L.vx1);
      dma9<PAD>(lg + slot * 18432 + 9216, L.sg, L.go + (k0 + 32) * 2752, vg);
    }
    mark = *(volatile LAS unsigned*)(size_t)fa;
    landed(F_FULL + t, mb, L.nz);
    slot = slot == 2 ? 0 : slot + 1;
    k0 = (k0 + 64) & 511;
  }
  TS(1);
  const i32x4 sd = mksrd(a.wd, DF * DM * 2);
  const unsigned wsb = mb * 16384 + g.nt * 128, lb = mb * 2048;
  const __amdgpu_buffer_rsrc_t rd = __builtin_amdgcn_make_buffer_rsrc((void*)a.wd, 0, DF * DM * 2, 0x00020000);
  const __amdgpu_buffer_rsrc_t ro = __builtin_amdgcn_make_buffer_rsrc((void*)a.wo, 0, DM * DM * 2, 0x00020000);
  u32x4 park[50];
  auto parks = [&](int i0, int i1) {
    UNROLL for (int i = i0; i < i1; ++i) {
      park[i] = __builtin_amdgcn_raw_buffer_load_b128(i < 18 ? rd : ro, vw, wsb + (w2row(34 + (i >> 1)) + (i & 1) * 8) * 1024, 0);
      CFENCE();
    }
  };
  if constexpr (ROLE == 1) {
    __builtin_amdgcn_s_setprio(0);
    parks(0, 50);
    TS(2);
    spin_lds(fa, 6); w2fill<8, 12, PAD_W>(lb, vw, sd, wsb); w2fill<21, 25, PAD_W>(lb, vw, sd, wsb);
    spin_lds(fa, 7); w2fill<0, 4, PAD_W>(lb, vw, sd, wsb); w2fill<12, 16, PAD_W>(lb, vw, sd, wsb);
    spin_lds(fa, 8);
    TS(3);
    w2fill<4, 8, PAD_E>(lb, vw, sd, wsb);
    landed(F_W0, mb, L.nz);
    TS(10);
    w2fill<16, 21, PAD_E>(lb, vw, sd, wsb); w2fill<25, 34, PAD_E>(lb, vw, sd, wsb);
  } else {
    const unsigned vh = __umul24(lane >> 2, 2752) + ((lane & 3) ^ ((lane >> 4) & 2)) * 16;
    auto g2 = [&](int t) {
      const unsigned b = (t < 3 ? 6 + t : t) * 16384, k = ((g.rot + t) & 7) * 64;
      dma4<PAD_GU>(b + mb * 4096, L.sa, g.g8 * 128 + (k + 32 * mb) * 3072, 8 * 3072, L.va0, L.va1);
      dma4<PAD_GU>(b + 8192 + mb * 4096, L.sg, L.go + k * 2752, 16 * 2752, vh, vh);
    };
    const unsigned l2 = min(lane, 58), b2 = 2 * (g.g8 * 16 + g.rt) + mb, i5 = 2 * ((g.g8 & 1) * 8 + (g.rt & 7)) + mb;
    g2(0);
    landed(F_FULL + 8, mb, L.nz);
    g2(1); g2(2);
    landed(F_FULL + 9, mb, L.nz);
    TS(5);
    if (!(XP & 4)) touch(l2 < 43 ? (const char*)a.wd + (b2 * 43 + l2) * 128 : (const char*)a.wo + (b2 * 16 + l2 - 43) * 128);
    spin_lds(fa, 7);
    g2(3); g2(5);
    spin_lds(fa, 8);
    g2(4);
    landed(F_FULL + 10, mb, L.nz);
    TS(6);
    if (!(XP & 8)) touch((l2 < 43 ? (const char*)a.wd + (i5 * 43 + l2) * 1024 : (const char*)a.wo + (i5 * 16 + l2 - 43) * 1024) + 384);
    parks(0, PARK0);
    spin_lds(fa, 10);
    g2(6); g2(7);
    landed(F_FULL + 11, mb, L.nz);
    TS(7);
    __builtin_amdgcn_s_setprio(0);
    parks(PARK0, 50);
    TS(2);
    lds_wait(C_K, 2); w2fill<0, 12, PAD_W>(lb, vw, sd, wsb);
    spin_lds(fa, 12); w2fill<12, 16, PAD_W>(lb, vw, sd, wsb);
    landed(F_W0, mb, L.nz);
    TS(10);
    w2fill<32, 34, PAD_W>(lb, vw, sd, wsb);
    spin_lds(fa, 14); w2fill<16, 24, PAD_W>(lb, vw, sd, wsb);
    spin_lds(fa, 16);
    TS(3);
    w2fill<24, 32, PAD_E>(lb, vw, sd, wsb);
  }
  landed(F_WB, mb, L.nz);
  wait_vm<0>();
  TS(4);
  __builtin_amdgcn_s_setprio(PARKW_PRIO);
  const unsigned pa = mb * 2048 + fresh_lane() * 16;
  spin_lds(fa, M_PRE);
  UNROLL for (int i = 0; i < 32; ++i) *(LAS u32x4*)(size_t)(pa + (i >> 1) * 4096 + (i & 1) * 1024) = park[i];
  lds_inc(C_W1);
  TS(8);
  spin_lds(fa, M_MID);
  UNROLL for (int i = 32; i < 50; ++i) *(LAS u32x4*)(size_t)(pa + (i >> 1) * 4096 + (i & 1) * 1024) = park[i];
  lds_inc(C_W2);
  TS(9);
}

template <int CTRL> DI float dadd(float v) {
  return opq(v + __uint_as_float(__builtin_amdgcn_update_dpp(0u, __float_as_uint(v), CTRL, 0xf, 0xf, true)));
}
template <int ROLE> DI void sider(const Args& a, const Geo& g, int mb, u64 epoch TS_ARG) {
  const int lane = fresh_lane();
  const unsigned i16 = lane & 15, gq = lane >> 4;
  char* ws = (char*)a.ws;
  const __amdgpu_buffer_rsrc_t rw = __builtin_amdgcn_make_buffer_rsrc(a.ws, 0, WS_END, 0x00020000);
  const f32x4 zero = {0.f, 0.f, 0.f, 0.f};
  const unsigned pe = g.ct * 16 + g.rt;
  auto pick = [&](int s) {
    u32x4 r;
    UNROLL for (int d = 0; d < 4; ++d) r[d] = i16 == 8 * s + 2 * d ? 0x3f80u : (i16 == 8 * s + 2 * d + 1 ? 0x3f800000u : 0u);
    return r;
  };
  u64 tag;
  if (PREWARM && mb == 1) {
    const unsigned gr = (g.g8 * 16 + g.rt) * 128, k = i16 >> 1;
    const unsigned o = k < 2 ? WS_FA + (g.tb64 * 8 + g.nt) * 256 + k * 128 : k == 2 ? WS_FC + pe * 128 : k == 3 ? WS_FT + (g.rt * 16 + g.ct) * 128 : (ROLE ? WS_FQ : WS_FK) + gr;
    asm volatile("s_mov_b64 exec, 0x3ff\n\tglobal_atomic_add %0, %1, %2 sc1\n\ts_mov_b64 exec, -1" ::"v"(o + (lane & 1) * 64), "v"(0u), "s"(ws) : "memory");
  }
  if ((ROLE ? SIDE_D1 : SIDE_D0) != 0) __builtin_amdgcn_s_sleep(ROLE ? SIDE_D1 : SIDE_D0);
  if (mb == 0) {
    u32x4 wv[16];
    const unsigned row = 32 * g.rt + gq;
    const char *p0, *p1;
    unsigned st1 = 4 * 2752;
    if constexpr (ROLE == 0) {
      p0 = (const char*)a.wqkv + row * 3072 + 1024 + (i16 >> 3) * 1024 + g.g8 * 128 + (i16 & 7) * 16;
      p1 = i16 < 8 ? p0 - 1024 : (const char*)(i16 < 12 ? a.wg : a.wu) + row * 2752 + g.u0 * 32 + (i16 & 3) * 16;
      st1 = i16 < 8 ? 4 * 3072 : 4 * 2752;
    } else {
      p0 = (const char*)a.wg + row * 2752 + (g.u0 + (g.g8 & 1)) * 32 + i16 * 16;
      p1 = (const char*)a.wu + row * 2752 + (g.u0 + (g.g8 & 1)) * 32 + i16 * 16;
    }
    TS(0);
    UNROLL for (int i = 0; i < 8; ++i) {
      if (XP & 1) { wv[i] = wv[8 + i] = u32x4{i16, gq, row, 1}; continue; }
      wv[i] = *(const GAS u32x4*)(p0 + i * 4 * (ROLE ? 2752 : 3072));
      wv[8 + i] = *(const GAS u32x4*)(p1 + i * st1);
    }
    SB();
    asm volatile("" : "+v"(epoch) :: "memory");
    tag = epoch + TAG_STEP;
    *(LAS u64*)(size_t)(L_CTL + 4 * C_TAG) = tag;
    SB();
    const u32x4 s0 = pick(0), s1 = pick(1);
    f32x4 c = zero;
    UNROLL for (int i = 0; i < 8; ++i) { c = mfma(s0, wv[i], c); c = mfma(s1, wv[8 + i], c); }
    st128(rw, WS_CSP + pe * 1024 + lane * 16, __builtin_bit_cast(u32x4, c));
    wait_vm<0>();
    set_flags<16>(ws, WS_FC + (g.ct * 16 + i16) * 128 + g.rt * 8, tag);
    TS(1);
    wait_free(ROLE ? GATHER_AT1 : GATHER_AT, lane);
    if (!(XP & 2)) poll<16>(ws, WS_FC + pe * 128 + i16 * 8, tag);
    if (!(XP & 2)) UNROLL for (int i = 0; i < 16; ++i) wv[i] = __builtin_amdgcn_raw_buffer_load_b128(rw, lane * 16, WS_CSP + (g.ct * 16 + i) * 1024, 16);
    SB();
    c = __builtin_bit_cast(f32x4, wv[0]);
    UNROLL for (int i = 1; i < 16; ++i) c = add4(c, __builtin_bit_cast(f32x4, wv[i]));
    asm volatile("" : "+v"(c));
    TS(7);
    wait_free(6, lane);
    *(LAS f32x4*)(size_t)(L_CS + (gq >> 1) * 512 + i16 * 32 + (gq & 1) * 16) = c;
  } else {
    u32x4 xv[8], qv[2];
    const unsigned tok = g.rt * 128 + g.ct * 8 + (lane >> 3);
    const char* px = (const char*)a.x + tok * 1024 + (lane & 7) * 16;
    const char* pq = (const char*)((lane & 2) ? a.wu : a.wg) + (32 * g.rt + (lane >> 2)) * 2752 + (g.u0 + ((g.g8 & 1) ? 0 : 8)) * 32 + (lane & 1) * 16;
    TS(0);
    UNROLL for (int i = 0; i < 8; ++i) xv[i] = (XP & 1) ? u32x4{i16, gq, tok, 1} : *(const GAS u32x4*)(px + i * 128);
    if constexpr (ROLE == 1) { UNROLL for (int i = 0; i < 2; ++i) qv[i] = (XP & 1) ? u32x4{i16, gq, tok, 1} : *(const GAS u32x4*)(pq + i * 16 * 2752); }
    SB();
    asm volatile("" : "+v"(epoch) :: "memory");
    tag = epoch + TAG_STEP;
    const s16x4 ones = {0x3f80, 0x3f80, 0x3f80, 0x3f80};
    f32x4 sm = zero, sq = zero;
    UNROLL for (int i = 0; i < 16; ++i) {
      const s16x4 v = __builtin_bit_cast(s16x4, u32x2{xv[i >> 1][2 * (i & 1)], xv[i >> 1][2 * (i & 1) + 1]});
      sm = __builtin_amdgcn_mfma_f32_4x4x4bf16_1k(ones, v, sm, 0, 0, 0);
      sq = __builtin_amdgcn_mfma_f32_4x4x4bf16_1k(v, v, sq, 0, 0, 0);
    }
    unsigned d = 0;
    UNROLL for (int i = 0; i < 4; ++i) d |= __float_as_uint(sq[i]) & ((lane & 3) == i ? ~0u : 0u);
    const float mean = dadd<0x141>(dadd<0x4E>(dadd<0xB1>(sm[0]))) * (1.f / DM);
    const float var = __builtin_fmaxf(opq(dadd<0x141>(dadd<0x4E>(dadd<0xB1>(__uint_as_float(d)))) * (1.f / DM)) - mean * mean, 0.f);
    const float den = rbf(__builtin_amdgcn_sqrtf(rbf(rbf(var) + 1e-5f)));
    const float rs = __builtin_amdgcn_rcpf(den);
    const f32x4 tc = {-rbf(mean), opq(GELU_C0 * rs), opq(-0.1029432397f * rs * rs * rs), opq(den * den)};
    st128(rw, (lane & 7) == 0 ? WS_STP + tok * 16 : WS_END, __builtin_bit_cast(u32x4, tc));
    if constexpr (ROLE == 1) {
      const u32x4 s = pick(0);
      f32x4 c = zero;
      UNROLL for (int i = 0; i < 2; ++i) c = mfma(s, qv[i], c);
      UNROLL for (int i = 0; i < 4; ++i) c[i] = dadd<0x128>(dadd<0x124>(c[i]));
      st128(rw, (lane & 44) == 0 ? WS_CQP + (g.g8 * 16 + g.rt) * 128 + i16 * 32 + gq * 16 : WS_END, __builtin_bit_cast(u32x4, c));
    }
    wait_vm<0>();
    set_flags<ROLE ? 32 : 16>(ws, gq ? WS_FQ + (g.g8 * 16 + i16) * 128 + g.rt * 8 : WS_FT + (g.rt * 16 + i16) * 128 + g.ct * 8, tag);
    TS(1);
    wait_free(ROLE ? GATHER_AT1 : GATHER_AT, lane);
    if (!(XP & 2)) poll<ROLE ? 32 : 16>(ws, (gq ? WS_FQ + (g.g8 * 16 + g.rt) * 128 : WS_FT + (g.rt * 16 + g.ct) * 128) + i16 * 8, tag);
    UNROLL for (int i = 0; i < 2; ++i) qv[i] = __builtin_amdgcn_raw_buffer_load_b128(rw, lane * 16, WS_STP + g.rt * 2048 + i * 1024, 16);
    f32x4 c = zero;
    if constexpr (ROLE == 1 && !(XP & 2)) { UNROLL for (int i = 0; i < 2; ++i) xv[i] = __builtin_amdgcn_raw_buffer_load_b128(rw, lane * 16, WS_CQP + g.g8 * 2048 + i * 1024, 16); }
    if constexpr (ROLE == 1) {
      c = add4(__builtin_bit_cast(f32x4, xv[0]), __builtin_bit_cast(f32x4, xv[1]));
      UNROLL for (int i = 0; i < 4; ++i) {
        unsigned u = __float_as_uint(dadd<0x128>(c[i]));
        { auto r = __builtin_amdgcn_permlane16_swap(u, u, false, false); u = __float_as_uint(opq(__uint_as_float(r[0]) + __uint_as_float(r[1]))); }
        { auto r = __builtin_amdgcn_permlane32_swap(u, u, false, false); c[i] = opq(__uint_as_float(r[0]) + __uint_as_float(r[1])); }
      }
    }
    asm volatile("" : "+v"(c), "+v"(qv[0]), "+v"(qv[1]));
    TS(7);
    wait_free(6, lane);
    UNROLL for (int i = 0; i < 2; ++i) {
      const unsigned t = 64 * i + lane;
      *(LAS u32x4*)(size_t)(L_TC + t * 16) = qv[i];
      if constexpr (ROLE == 0) {
        *(LAS unsigned*)(size_t)(L_NM + t * 4) = qv[i][0];
        *(LAS float*)(size_t)(L_RS + t * 4) = opq(__uint_as_float(qv[i][1]) * (1.f / GELU_C0));
      }
    }
    if constexpr (ROLE == 1) *(LAS f32x4*)(size_t)(L_CS + 1024 + (lane & 7) * 16) = c;
  }
  CFENCE(); *(volatile LAS unsigned*)(size_t)(L_LND + 8 * F_RDY + 4 * mb) = 1u; CFENCE();
  TS(2);
  if constexpr (ROLE == 0) {
    const unsigned kb = g.g8 * 16 + g.rt;
    lds_wait(C_X, 4);
    TS(3);
    UNROLL for (int j = 0; j < 2; ++j) {
      const unsigned o = (2 * mb + j) * 1024 + lane * 16;
      const f32x4 v = add4(add4(lds128f(L_KX + o), lds128f(L_KX + 4096 + o)), add4(lds128f(L_KX + 8192 + o), lds128f(L_KX + 12288 + o)));
      st64(rw, WS_KP + kb * 2048 + (2 * mb + j) * 512 + (gq >> 1) * 256 + i16 * 16 + (gq & 1) * 8, u32x2{pk2(lo2(v)), pk2(hi2(v))});
    }
    const unsigned zo = mb * 128 + (lane & 7) * 16;
    st128(rw, lane < 8 ? WS_ZP + kb * 256 + zo : WS_END,
          __builtin_bit_cast(u32x4, add4(add4(lds128f(L_ZX + zo), lds128f(L_ZX + 256 + zo)), add4(lds128f(L_ZX + 512 + zo), lds128f(L_ZX + 768 + zo)))));
    publish<2>(C_K, ws, WS_FK + (g.g8 * 16 + i16) * 128 + g.rt * 8, tag);
    TS(5);
    wait_free(KV_AT, lane);
    poll<16>(ws, WS_FK + kb * 128 + i16 * 8, tag);
    TS(4);
    __builtin_amdgcn_s_setprio(GATHER_PRIO);
    u32x4 pv[16], zv[2];
    UNROLL for (int j = 0; j < 2; ++j)
      zv[j] = __builtin_amdgcn_raw_buffer_load_b128(rw, i16 * 256 + gq * 16, WS_ZP + g.g8 * 4096 + (2 * mb + j) * 64, 16);
    UNROLL for (int i = 0; i < 16; ++i)
      pv[i] = __builtin_amdgcn_raw_buffer_load_b128(rw, gq * 2048 + i16 * 16 + (i & 3) * 256, WS_KP + (g.g8 * 16 + (i & 12)) * 2048 + mb * 1024, 16);
    SB();
    const u32x4 s0 = pick(0), s1 = pick(1);
    UNROLL for (int j = 0; j < 2; ++j) {
      f32x4 z = __builtin_bit_cast(f32x4, zv[j]);
      UNROLL for (int i = 0; i < 4; ++i) z[i] = dadd<0x140>(dadd<0x141>(dadd<0x4E>(dadd<0xB1>(z[i]))));
      *(LAS u32x2*)(size_t)(L_KF + 2048 + (2 * mb + j) * 32 + gq * 8) = u32x2{pk2(lo2(z)), pk2(hi2(z))};
    }
    SB();
    f32x4 sum[2] = {zero, zero};
    UNROLL for (int i = 0; i < 16; ++i) sum[(i >> 1) & 1] = mfma((i & 1) ? s1 : s0, pv[i], sum[(i >> 1) & 1]);
    UNROLL for (int j = 0; j < 2; ++j) *(LAS u32x2*)(size_t)(L_KF + (2 * mb + j) * 512 + lane * 8) = u32x2{pk2(lo2(sum[j])), pk2(hi2(sum[j]))};
    __builtin_amdgcn_s_setprio(3);
    lds_inc(C_KF);
    TS(8);
  }
  if (mb == 0) {
    if constexpr (ROLE == 1) lds_wait(C_A0, 4); else wait_free(16, lane);
    poll_levels(ws, WS_FA + (g.tb64 * 8 + g.nt) * 256 + (lane & 31) * 8, tag);
    TS(6);
  } else if (blockIdx.x == 0) {
    NOUNROLL for (int k = 0; k < 4; ++k) {
      poll<64>(ws, WS_FT + (k * 4 + gq) * 2048 + i16 * 8, tag);
      poll<64>(ws, WS_FC + (k * 4 + gq) * 2048 + i16 * 8, tag);
    }
    set_flags<1>(ws, WS_EPOCH, tag);
  }
}

#define ENSURE_FULL(c, d) ensure2(pk_, ba, c)
struct PF {
  static constexpr int NF = 10, D = PFD, NG = 16 * NF;
  f32x4 acc[NF][4];
  u32x4 xf[2][4];
  u32x2 wl[D + 1], wh[D + 1];
  unsigned xa[2][2], ga[2][2], gs[2][2];
  unsigned pk_, fa, ba;
  f32x4 ec[10];
  unsigned ea[2], eb, ed;
  template <int I> DI void lde() {
    if constexpr (I < 4) ec[4 + I] = lds128f(eb + (I & 1) * 64 + (I >> 1) * 512);
    else if constexpr (I < 6) ec[4 + I] = lds128f(ed + (I & 1) * 64);
    else if constexpr (I >= 2 * D) ec[I - 2 * D] = lds128f(ea[(I - 2 * D) >> 1] + (I & 1) * 256);
  }
  template <int G, int T> DI void ldw() {
    constexpr int kk = G / NF, f = G % NF, c = f % 5 - 1;
    if constexpr (G < NG) {
      const unsigned ad = (c < 0 ? gs[f == 5][T] : ga[c >> 1][T] + (f > 5) * 64 + (c & 1) * 32) + ((kk >> 1) % 3) * 18432 + (kk & 1) * 9216;
      if constexpr (T == 0) wl[G % (D + 1)] = ldstr(ad); else wh[G % (D + 1)] = ldstr(ad);
    } else lde<2 * (G - NG) + T>();
  }
  template <int KK, int TB> DI void ldx() {
    if constexpr (KK < 16) xf[KK & 1][TB] = lds128(xa[KK & 1][TB >> 1] + ((KK >> 1) % 3) * 16384 + (TB & 1) * 2048);
    else lde<2 * D + TB>();
  }
  template <int G, int TB> DI void mm() {
    mfma_k<(G < NF)>(acc[G % NF][TB], cat(wl[G % (D + 1)], wh[G % (D + 1)]), xf[(G / NF) & 1][TB]); SB();
  }
  template <int G> DI void step(unsigned& ts) {
    if constexpr (G < NG) {
      constexpr int kk = G / NF, f = G % NF, s = kk >> 1, nxt = s == 7 ? F_RDY : F_FULL + s + 1;
      constexpr int FC = NF - D;
      static_assert(D == 3 || D == 4, "x reads ride in wide fragments");
      if constexpr ((kk & 1) && f == FC) { ENSURE_FULL(nxt, s); SB(); }
      mm<G, 0>();
      ldw<G + D, 0>(); SB();
      mm<G, 1>();
      ldw<G + D, 1>(); SB();
      if constexpr (f % 5 != 0) {
        mm<G, 2>();
        if constexpr (f == FC || f == FC + 1) ldx<kk + 1, 2 * (f - FC)>();
        if constexpr ((kk & 1) && f == 4) pk_ = peek2(ba, nxt);
        SB();
      }
      if constexpr ((kk & 1) && f == FC - 1) {
        CFENCE(); *(volatile LAS unsigned*)(size_t)fa = s + 1; CFENCE(); SB();
      }
      if constexpr (f % 5 != 0) {
        mm<G, 3>();
        if constexpr (f == FC || f == FC + 1) ldx<kk + 1, 2 * (f - FC) + 1>();
        SB();
      }
      step<G + 1>(ts);
    }
  }
  template <int G> DI void pre() { if constexpr (G < D) { ldw<G, 0>(); ldw<G, 1>(); pre<G + 1>(); } }
};

struct PK {
  static constexpr int NF = 8, D = PFK, NG = 16 * NF;
  f32x4 acc[NF][2];
  u32x4 xf[16][2];
  u32x2 wl[D + 1], wh[D + 1];
  unsigned xa[2], pa[4], pg[2], pa2[4], pg2[2];
  unsigned pk_, fa, ba;
  f32x4 ec[10];
  unsigned ea, eb;
  template <int GRP, int I> DI void lde() {
    if constexpr (GRP == 0) {
      if constexpr (I < 4) ec[I] = lds128f(ea + (I & 1) * 64 + (I >> 1) * 512);
      else if constexpr (I < 12) ec[4 + ((I - 4) >> 2)][I & 3] = *(const LAS float*)(size_t)(eb + (I - 4) * 64);
    } else {
      if constexpr (I < 2) ec[I] = lds128f(ea + I * 256);
      else if constexpr (I < 10) ec[I] = lds128f(eb + (I - 2) * 64);
    }
  }
  template <int GRP, int G, int T> DI void ldw() {
    constexpr int kk = G / NF, f = G % NF;
    if constexpr (G < NG) {
      constexpr int s = kk >> 1, hi = s < 3 || s >= 6;
      unsigned ad;
      if constexpr (GRP == 0) ad = pa[f & 3] + (s % 3) * 16384 + (f >> 2) * 8192 + (kk & 1) * 4096 + T * 512;
      else if constexpr (f < 4) ad = (hi ? pa2[f] : pa[f]) + (s % 3) * 16384 + (kk & 1) * 4096 + T * 512;
      else ad = (hi ? pg2[f & 1] : pg[f & 1]) + (s % 3) * 16384 + ((f >> 1) & 1) * 4096 + (kk & 1) * 2048 + T * 256;
      if constexpr (T == 0) wl[G % (D + 1)] = ldstr(ad); else wh[G % (D + 1)] = ldstr(ad);
    } else lde<GRP, 2 * (G - NG) + T>();
  }
  template <int GRP, int KK, int TB> DI void ldx() {
    if constexpr (KK == 16) lde<GRP, 2 * D + TB>();
    else if constexpr (GRP == 0) xf[KK][TB] = lds128(xa[KK & 1] + ((KK >> 1) % 3) * 16384 + TB * 2048);
  }
  template <int GRP, int G, int TB> DI void mm() {
    const u32x4 wf = cat(wl[G % (D + 1)], wh[G % (D + 1)]);
    if constexpr (GRP == 0) mfma_k<(G < NF)>(acc[G % NF][TB], xf[G / NF][TB], wf);
    else mfma_k<(G < NF)>(acc[G % NF][TB], wf, xf[G / NF][TB]);
    SB();
  }
  template <int GRP, int G> DI void step(unsigned& ts) {
    if constexpr (G < NG) {
      constexpr int kk = G / NF, f = G % NF, s = 8 * GRP + (kk >> 1);
      constexpr int nxt = s < 7 ? F_FULL + s + 1 : s == 7 ? F_RDY : s == 8 ? F_FULL + 9 : s == 10 ? F_FULL + 10 : s == 13 ? F_FULL + 11 : -1;
      constexpr int FC = NF - D;
      static_assert(D >= 5 && D <= 6, "constant slots");
      if constexpr ((kk & 1) && f == 0 && nxt >= 0) { pk_ = peek2(ba, nxt); SB(); }
      if constexpr ((kk & 1) && f == FC) {
        if constexpr (s < 8 || (s & 1)) { CFENCE(); *(volatile LAS unsigned*)(size_t)fa = s + 1; CFENCE(); SB(); }
        if constexpr (nxt >= 0) { ENSURE_FULL(nxt, s); SB(); }
      }
      mm<GRP, G, 0>();
      ldw<GRP, G + D, 0>();
      if constexpr (f == FC || f == FC + 1) ldx<GRP, kk + 1, f - FC>();
      SB();
      mm<GRP, G, 1>();
      ldw<GRP, G + D, 1>(); SB();
      step<GRP, G + 1>(ts);
    }
  }
  template <int GRP, int G> DI void pre() { if constexpr (G < D) { ldw<GRP, G, 0>(); ldw<GRP, G, 1>(); pre<GRP, G + 1>(); } }
};

#define EACH4 UNROLL for (int i = 0; i < 4; ++i)
DI f32x2 half(const f32x4& a, const f32x4& b, int i) { return i == 0 ? lo2(a) : i == 1 ? hi2(a) : i == 2 ? lo2(b) : hi2(b); }
DI u32x4 geglu8(const f32x4& g0, const f32x4& g1, const f32x4& u0, const f32x4& u1, const f32x4& cg0, const f32x4& cg1,
                const f32x4& cu0, const f32x4& cu1, const f32x4& t0, const f32x4& t1) {
  f32x2 g[4], u[4], a[4];
  u32x4 r;
  auto T = [&](int i, int k) { return sp(i < 2 ? t0[k] : t1[k]); };
  EACH4 g[i] = fma2(half(cg0, cg1, i), T(i, 0), half(g0, g1, i));
  SB(); EACH4 a[i] = g[i] * g[i];
  SB(); EACH4 a[i] = fma2(a[i], T(i, 2), T(i, 1));
  SB(); EACH4 a[i] = a[i] * g[i];
  SB(); EACH4 u[i] = fma2(half(cu0, cu1, i), T(i, 0), half(u0, u1, i));
  SB(); EACH4 a[i] = f32x2{__builtin_amdgcn_exp2f(a[i][0]), __builtin_amdgcn_exp2f(a[i][1])};
  SB(); EACH4 a[i] = fma2(a[i], T(i, 3), T(i, 3));
  SB(); EACH4 u[i] = u[i] * g[i];
  SB(); EACH4 a[i] = f32x2{__builtin_amdgcn_rcpf(a[i][0]), __builtin_amdgcn_rcpf(a[i][1])};
  SB(); EACH4 u[i] = u[i] * a[i];
  SB(); EACH4 r[i] = pk2(u[i]);
  SB();
  return r;
}
DI u32x4 phi8(const f32x2 (&c)[4], const f32x2 (&x)[4], const f32x2 (&y)[4], const f32x2 (&rs)[4]) {
  f32x2 b[4], e[4];
  u32x4 r;
  EACH4 b[i] = fma2(x[i], y[i], c[i]);
  SB(); EACH4 b[i] = fma2(b[i], rs[i], sp(1e-6f));
  SB(); EACH4 e[i] = b[i] * sp(1.44269504f);
  SB(); EACH4 b[i] = f32x2{__builtin_fmaxf(b[i][0], 1e-6f), __builtin_fmaxf(b[i][1], 1e-6f)};
  SB(); EACH4 e[i] = f32x2{__builtin_amdgcn_fmed3f(__builtin_amdgcn_exp2f(e[i][0]), 0.f, 1.f), __builtin_amdgcn_fmed3f(__builtin_amdgcn_exp2f(e[i][1]), 0.f, 1.f)};
  SB(); EACH4 b[i] = b[i] + e[i];
  SB(); EACH4 r[i] = pk2(b[i]);
  SB();
  return r;
}

DI void phase1_kv(const Args& a, const Geo& g, int w TS_ARG) {
  PK P;
  unsigned ts = 0;
  {
    const int lane = fresh_lane();
    const unsigned i16 = lane & 15, gq = lane >> 4, q = (lane >> 2) & 3, p = lane & 3;
    P.fa = L_FREE + w * 256 + lane * 4; P.ba = both_addr(lane);
    asm volatile("" : "+v"(P.ba));
    UNROLL for (int ks = 0; ks < 2; ++ks) {
      P.xa[ks] = (32 * w + i16) * 128 + (((4 * ks + gq) ^ (i16 >> 1)) * 16);
      asm volatile("" : "+v"(P.xa[ks]));
    }
    const unsigned sw = (q >> 1) | ((gq & 1) << 1);
    UNROLL for (int j = 0; j < 4; ++j) {
      P.pa[j] = L_A + (8 * gq + q) * 128 + ((j ^ sw) * 32) + p * 8;
      asm volatile("" : "+v"(P.pa[j]));
    }
    P.ea = L_NM + (32 * w + 4 * gq) * 4; P.eb = L_CS + i16 * 4;
    asm volatile("" : "+v"(P.ea), "+v"(P.eb), "+v"(P.fa));
  }
  TS(0);
  spin_lds(P.ba + 8 * F_FULL, 1);
  TS(1);
  P.ldx<0, 0, 0>(); P.ldx<0, 0, 1>();
  P.pre<0, 0>();
  SB();
  P.step<0, 0>(ts);
  mfma_done();
  TS(2);
  const int lane = fresh_lane();
  {
    const u32x4 ones = {0x3f803f80u, 0x3f803f80u, 0x3f803f80u, 0x3f803f80u};
    const f32x4 zero = {0.f, 0.f, 0.f, 0.f};
    f32x2 nm[4], rs[4];
    EACH4 { nm[i] = half(P.ec[0], P.ec[1], i); rs[i] = half(P.ec[2], P.ec[3], i); }
    f32x4 kv, zz;
    UNROLL for (int j = 0; j <= 4; ++j) {
      u32x4 pk, pv;
      if (j < 4) {
        f32x2 v[4], y[4];
        EACH4 v[i] = fma2(nm[i], sp(P.ec[5][j]), half(P.acc[4 + j][0], P.acc[4 + j][1], i));
        SB(); EACH4 v[i] = v[i] * rs[i];
        SB(); EACH4 pv[i] = pk2(v[i]);
        SB();
        EACH4 { v[i] = half(P.acc[j][0], P.acc[j][1], i); y[i] = sp(P.ec[4][j]); }
        pk = phi8(v, nm, y, rs);
      }
      if (j > 0) {
        *(LAS f32x4*)(size_t)(L_KX + w * 4096 + (j - 1) * 1024 + lane * 16) = kv;
        *(LAS f32x4*)(size_t)(L_ZX + w * 256 + (j - 1) * 64 + (lane >> 4) * 16) = zz;
      }
      if (j == 2) P.pk_ = peek2(P.ba, F_FULL + 8);
      if (j < 4) { kv = mfma(pk, pv, zero); zz = mfma(pk, ones, zero); }
      if (j == 3) {
        SB();
        UNROLL for (int k = 0; k < 4; ++k) { P.pa2[k] = P.pa[k] + 49152; asm volatile("" : "+v"(P.pa2[k])); }
        UNROLL for (int k = 0; k < 2; ++k) {
          P.pg[k] = L_A + 8192 + (8 * (lane >> 4) + ((lane >> 2) & 3)) * 64 + ((k ^ ((lane >> 4) & 1)) * 32) + (lane & 3) * 8;
          P.pg2[k] = P.pg[k] + 49152;
          asm volatile("" : "+v"(P.pg[k]), "+v"(P.pg2[k]));
        }
        ensure2(P.pk_, P.ba, F_FULL + 8);
        P.pre<1, 0>();
      }
      SB();
    }
    lds_inc(C_X);
  }
  P.ea = L_TC + (32 * w + (lane & 15)) * 16; P.eb = L_CS + 512 + (lane >> 4) * 16;
  unsigned sa = WS_ACT + ((g.rt * 8 + 2 * w) * NSTEP + 34 + g.g8) * 1024 + lane * 16;
  asm volatile("" : "+v"(P.ea), "+v"(P.eb), "+v"(sa));
  TS(7);
  SB();
  P.step<1, 0>(ts);
  mfma_done();
  TS(3);
  const __amdgpu_buffer_rsrc_t rw = __builtin_amdgcn_make_buffer_rsrc(a.ws, 0, WS_END, 0x00020000);
  const u64 tag = get_tag();
  P.pk_ = peek(C_KF);
  SB();
  UNROLL for (int tb = 0; tb < 2; ++tb) {
    st128(rw, sa + tb * (NSTEP * 1024),
          geglu8(P.acc[4][tb], P.acc[5][tb], P.acc[6][tb], P.acc[7][tb], P.ec[6], P.ec[7], P.ec[8], P.ec[9], P.ec[tb], P.ec[tb]));
    SB();
  }
  TS(4);
  const int ln = fresh_lane();
  u32x4 pq[4];
  UNROLL for (int k = 0; k < 4; ++k) {
    const int hp = k >> 1, tb = k & 1;
    f32x2 v[4], x[4], y[4], rs[4];
    const float r1 = P.ec[tb][1] * (1.f / GELU_C0);
    EACH4 { v[i] = half(P.acc[2 * hp][tb], P.acc[2 * hp + 1][tb], i); x[i] = half(P.ec[2 + 2 * hp], P.ec[3 + 2 * hp], i); y[i] = sp(P.ec[tb][0]); rs[i] = sp(r1); }
    pq[k] = phi8(v, x, y, rs);
    asm volatile("" : "+v"(pq[k]));
  }
  publish<4>(C_A0 + 2, (char*)a.ws, WS_FA + (g.rt * 16 + (ln & 15)) * 256 + 128 + g.g8 * 8, tag);
  TS(8);
  ensure(P.pk_, C_KF, 2);
  u32x2 kf[4], zf[4];
  UNROLL for (int j = 0; j < 4; ++j) {
    zf[j] = *(const LAS u32x2*)(size_t)(L_KF + 2048 + j * 32 + (ln >> 4) * 8);
    kf[j] = *(const LAS u32x2*)(size_t)(L_KF + j * 512 + ln * 8);
  }
  const f32x4 zero = {0.f, 0.f, 0.f, 0.f};
  f32x4 num[4][2], den[4][2];
  UNROLL for (int k = 0; k < 4; ++k)
    UNROLL for (int j = 0; j < 2; ++j) den[k][j] = mfma_k16(zf[(k & 2) + j], u32x2{pq[k][2 * j], pq[k][2 * j + 1]}, zero);
  SB();
  float rd[4][2];
  UNROLL for (int k = 0; k < 4; ++k)
    UNROLL for (int j = 0; j < 2; ++j) {
      num[k][j] = mfma_k16(kf[(k & 2) + j], u32x2{pq[k][2 * j], pq[k][2 * j + 1]}, zero);
      rd[k][j] = __builtin_amdgcn_rcpf(opq(den[k][j][0] + 1e-6f));
      SB();
    }
  UNROLL for (int k = 0; k < 4; ++k) {
    u32x4 o;
    f32x2 v[4];
    EACH4 v[i] = half(num[k][0], num[k][1], i) * sp(rd[k][i >> 1]);
    SB(); EACH4 o[i] = pk2(v[i]);
    st128(rw, sa + (k & 1) * (NSTEP * 1024) + (9 + g.g8 + (k >> 1)) * 1024, o);
    SB();
  }
  TS(5);
}

DI void phase1_ffn(const Args& a, const Geo& g, int w TS_ARG) {
  PF P;
  unsigned ts = 0;
  const int m = w >> 1, n = w & 1;
  unsigned sa[2];
  {
    const int lane = fresh_lane();
    const unsigned i16 = lane & 15, gq = lane >> 4, q = (lane >> 2) & 3, p = lane & 3;
    P.fa = L_FREE + w * 256 + lane * 4; P.ba = both_addr(lane);
    asm volatile("" : "+v"(P.ba));
    UNROLL for (int j = 0; j < 2; ++j) {
      UNROLL for (int hf = 0; hf < 2; ++hf) P.xa[j][hf] = (64 * m + 32 * (hf ^ n) + i16) * 128 + (((4 * j + gq) ^ (i16 >> 1)) * 16);
      const unsigned b = ((8 * gq + 4 * j + q) ^ ((gq & 1) << 2)) * 288 + p * 8;
      P.gs[0][j] = L_G + b + 256; P.gs[1][j] = L_U + b + 256;
      P.ga[0][j] = L_G + b + n * 128; P.ga[1][j] = L_U + b + n * 128;
      sa[j] = WS_ACT + ((g.rt * 8 + 4 * m + 2 * (j ^ n)) * NSTEP + 2 * g.g8 + n) * 1024 + lane * 16;
      P.ea[j] = L_TC + (64 * m + 32 * (j ^ n) + i16) * 16;
      asm volatile("" : "+v"(P.xa[j][0]), "+v"(P.xa[j][1]), "+v"(sa[j]), "+v"(P.ea[j]));
      asm volatile("" : "+v"(P.gs[0][j]), "+v"(P.gs[1][j]), "+v"(P.ga[0][j]), "+v"(P.ga[1][j]));
    }
    P.ed = L_CS + 1024 + gq * 16; P.eb = L_CS + gq * 16 + n * 256;
    asm volatile("" : "+v"(P.eb), "+v"(P.ed), "+v"(P.fa));
  }
  TS(0);
  spin_lds(P.ba + 8 * F_FULL, 1);
  TS(1);
  P.ldx<0, 0>(); P.ldx<0, 1>(); P.ldx<0, 2>(); P.ldx<0, 3>();
  P.pre<0>();
  SB();
  P.step<0>(ts);
  mfma_done();
  TS(3);
  const __amdgpu_buffer_rsrc_t rw = __builtin_amdgcn_make_buffer_rsrc(a.ws, 0, WS_END, 0x00020000);
  auto sto = [&](int tb) { return sa[tb >> 1] + (tb & 1) * (NSTEP * 1024); };
  const f32x4* t = P.ec;
  f32x4 e2[4];
  u64 tag;
  UNROLL for (int tb = 0; tb < 4; ++tb) {
    st128(rw, sto(tb), geglu8(P.acc[1][tb], P.acc[2][tb], P.acc[3][tb], P.acc[4][tb], P.ec[4], P.ec[5], P.ec[6], P.ec[7], t[tb], t[tb]));
    if (tb == 1) {
      UNROLL for (int i = 0; i < 4; ++i) e2[i] = lds128f(P.eb + 128 + (i & 1) * 64 + (i >> 1) * 512);
      tag = get_tag();
    }
    SB();
  }
  TS(4);
  u32x4 hs[HOLD];
  UNROLL for (int tb = 0; tb < HOLD; ++tb) {
    hs[tb] = geglu8(P.acc[6][tb], P.acc[7][tb], P.acc[8][tb], P.acc[9][tb], e2[0], e2[1], e2[2], e2[3], t[tb], t[tb]);
    asm volatile("" : "+v"(hs[tb]));
  }
  const int lane = fresh_lane();
  publish<4>(C_A0, (char*)a.ws, WS_FA + (g.rt * 16 + (lane & 15)) * 256 + g.g8 * 8, tag);
  TS(7);
  UNROLL for (int tb = 0; tb < HOLD; ++tb) st128(rw, sto(tb) + 16 * 1024, hs[tb]);
  SB();
  UNROLL for (int tb = HOLD; tb < 4; ++tb) {
    st128(rw, sto(tb) + 16 * 1024, geglu8(P.acc[6][tb], P.acc[7][tb], P.acc[8][tb], P.acc[9][tb], e2[0], e2[1], e2[2], e2[3], t[tb], t[tb]));
    SB();
  }
  {
    const u32x4 r = geglu8(P.acc[0][0], P.acc[0][1], P.acc[5][0], P.acc[5][1], P.ec[8], P.ec[8], P.ec[9], P.ec[9], t[0], t[1]);
    const unsigned o = (g.g8 < 6 ? WS_ACT + ((g.rt * 8 + 4 * m + 2 * n) * NSTEP + (g.g8 < 4 ? 32 + (g.g8 >> 1) : 42)) * 1024 + (g.g8 & 1) * 512 : WS_END) + lane * 8;
    st64(rw, o, u32x2{r[0], r[1]});
    st64(rw, o + NSTEP * 1024, u32x2{r[2], r[3]});
  }
  TS(5);
}

DI void swap16(unsigned& a, unsigned& b) { auto r = __builtin_amdgcn_permlane16_swap(a, b, false, false); a = r[0]; b = r[1]; }
struct P2 {
  static constexpr int NW = 30, PW = P2_PW, PRE = 8, DH = 3;
  f32x4 f[2][4], s[2][4], fx[4];
  f32x2 sg[4][2], r[8];
  u32x4 av[2][NW], xr[2];
  unsigned pf, ps, pw, po;
  u32x2 wl[NW][4], wh[NW][4];
  unsigned wa[3][4], va[2], pk_, xa, la, ba;
  __amdgpu_buffer_rsrc_t rw;
  int w, kh;
  template <int I, int B> DI void lda() {
    if constexpr (I == 16 || I == 21) {
      const unsigned v = va[B] - (la >> 1) + (I == 16 ? 0 : kh << 30);
      av[B][I] = cat(__builtin_amdgcn_raw_buffer_load_b64(rw, v, I * 2048, 16), __builtin_amdgcn_raw_buffer_load_b64(rw, v + 512, I * 2048, 16));
    }
    else if constexpr (I < 22) av[B][I] = __builtin_amdgcn_raw_buffer_load_b128(rw, va[B] + (I & 1) * 2048, (I >> 1) * 4096, 16);
    else if constexpr (I < NW) av[B][I] = __builtin_amdgcn_raw_buffer_load_b128(rw, va[B] + (I & 1) * 2048, (I >> 1) * 4096 - 1024, 16);
  }
  template <int I, int NB, int T> DI void ldw() {
    if constexpr (I < NW) {
      constexpr int reg = I < 17 ? 2 * I : (I < 22 ? 2 * I - 34 : 2 * I - 35);
      const unsigned ad = wa[reg >> 4][NB] + (reg & 15) * 4096 + T * 2048;
      if constexpr (T == 0) wl[I][NB] = ldstr(ad); else wh[I][NB] = ldstr(ad);
    }
  }
  template <int H, int K> DI void ldh() { if constexpr (H >= 2 * PRE) ldw<(H >> 1), 2 * (H & 1) + (K >> 1), (K & 1)>(); }
  template <int I, int NB, int B> DI void mm() {
    mfma_k<I == 0 || I == 22>(I < 22 ? f[B][NB] : s[B][NB], cat(wl[I][NB], wh[I][NB]), av[B][I]); SB();
  }
  template <int H> DI void need() {
    constexpr int I = H >> 1, NB = 2 * (H & 1);
    if constexpr (H & 1) asm volatile("" ::"v"(cat(wl[I][NB + 1], wh[I][NB + 1])));
    else asm volatile("" ::"v"(cat(wl[I][NB + 1], wh[I][NB + 1])), "v"(av[1][I]));
    SB();
  }
  DI void sig(unsigned v) { CFENCE(); *(volatile LAS unsigned*)(size_t)xa = v; CFENCE(); }
  DI void putf(int nb) { *(LAS f32x4*)(size_t)(L_FX + w * 4096 + nb * 1024 + la) = f[1][nb]; SB(); }
  DI void puts(int nb) { *(LAS f32x4*)(size_t)(pw + nb * 1024) = s[1][nb]; SB(); }
  static constexpr int H_L2 = 2 * (8 - PW), H_WB = 2 * PRE - DH, H_L3 = 2 * (17 - PW), H_W1 = 34 - DH, H_L4 = 2 * (22 - PW),
                       H_W2 = 50 - DH, H_FX = 51;
  template <int H> DI void half(unsigned& ts) {
    constexpr int I = H >> 1, NB = 2 * (H & 1);
    if constexpr (H == H_L2) { ensure(pk_, C_LVL, 2); TS(11); }
    if constexpr (H == H_WB) ensure2(pk_, ba, F_WB);
    if constexpr (H == H_L3) { ensure(pk_, C_LVL, 3); TSD(30); }
    if constexpr (H == H_W1) ensure(pk_, C_W1, 2);
    if constexpr (H == H_L4) { ensure(pk_, C_LVL, 4); TS(12); }
    if constexpr (H == H_W2) ensure(pk_, C_W2, 2);
    if constexpr (H == H_FX) {
      if (__builtin_expect(__any(pk_ < M_FX), 0)) wait_free(M_FX, la >> 4);
      CFENCE();
      UNROLL for (int nb = 0; nb < 4; ++nb) fx[nb] = lds128f(L_FX + (w ^ 1) * 4096 + nb * 1024 + la);
    }
    if constexpr (H == H_FX + 2) {
      f32x2 v[8], d[8];
      UNROLL for (int i = 0; i < 8; ++i) v[i] = (i & 1) ? hi2(f[0][i >> 1]) + hi2(fx[i >> 1]) : lo2(f[0][i >> 1]) + lo2(fx[i >> 1]);
      SB(); UNROLL for (int i = 0; i < 8; ++i) d[i] = v[i] * sp(-1.44269504f);
      SB(); UNROLL for (int i = 0; i < 8; ++i) d[i] = f32x2{__builtin_amdgcn_exp2f(d[i][0]), __builtin_amdgcn_exp2f(d[i][1])};
      SB(); UNROLL for (int i = 0; i < 8; ++i) d[i] = d[i] + sp(1.f);
      SB(); UNROLL for (int i = 0; i < 8; ++i) d[i] = f32x2{__builtin_amdgcn_rcpf(d[i][0]), __builtin_amdgcn_rcpf(d[i][1])};
      SB(); UNROLL for (int i = 0; i < 8; ++i) sg[i >> 1][i & 1] = v[i] * d[i];
    }
    SB();
    if constexpr (H < 2 * NW - 4) {
      need<H>();
      mm<I, NB, 0>();
      lda<I + PW, (H & 1)>();
      if constexpr (H == 50 || H == 51) {
        unsigned a0 = xr[H - 50][0], a1 = xr[H - 50][1], a2 = xr[H - 50][2], a3 = xr[H - 50][3];
        swap16(a0, a2); swap16(a1, a3);
        xr[H - 50] = u32x4{a0, a1, a2, a3};
        asm volatile("" : "+v"(xr[H - 50]));
      }
      if constexpr (H >= 52) {
        UNROLL for (int k = 2 * (H - 52); k < 2 * (H - 51); ++k) {
          r[k] = f32x2{bflo(xr[k >> 2][k & 3]), bfhi(xr[k >> 2][k & 3])};
          asm volatile("" : "+v"(r[k]));
        }
      }
      if constexpr (H == 20) {
        pw = L_SX + w * 4096 + la; ps = pw ^ 4096; po = L_CTL + 4 * (C_P0 + w); pf = L_CTL + 4 * (C_P0 + (w ^ 1));
        asm volatile("" : "+v"(pw), "+v"(ps), "+v"(po), "+v"(pf));
      }
      SB();
      mm<I, NB, 1>();
      ldh<H + DH, 0>(); ldh<H + DH, 1>();
      if constexpr (H < 8) { wa[1 + (H >> 2)][H & 3] = wa[0][H & 3] + (1 + (H >> 2)) * 65536; asm volatile("" : "+v"(wa[1 + (H >> 2)][H & 3])); }
      SB();
      mm<I, NB + 1, 0>();
      ldh<H + DH, 2>();
      if constexpr (H + 2 == H_L2 || H + 2 == H_L3 || H + 2 == H_L4) pk_ = peek(C_LVL);
      if constexpr (H + 2 == H_WB) pk_ = peek2(ba, F_WB);
      if constexpr (H + 2 == H_W1) pk_ = peek(C_W1);
      if constexpr (H + 2 == H_W2) pk_ = peek(C_W2);
      if constexpr (H + 2 == H_FX) { CFENCE(); pk_ = *(volatile LAS unsigned*)(size_t)(L_FREE + ((la >> 4) & 3) * 256); CFENCE(); }
      SB();
      mm<I, NB + 1, 1>();
      ldh<H + DH, 3>(); SB();
      if constexpr (H + DH == 25) sig(M_MID);
      if constexpr (H >= 44 && H < 48) putf(H - 44);
      if constexpr (H == 47) sig(M_FX);
      SB();
      half<H + 1>(ts);
    } else {
      need<56>();
      mm<28, 0, 1>(); ldh<59, 0>(); ldh<59, 1>(); SB();
      mm<28, 1, 1>(); ldh<59, 2>(); ldh<59, 3>(); SB();
      mm<28, 2, 1>(); mm<28, 3, 1>();
      mm<29, 0, 1>(); mm<29, 1, 1>(); mm<29, 2, 1>(); mm<29, 3, 1>();
      mm<28, 0, 0>(); mm<28, 1, 0>(); mm<28, 2, 0>(); mm<28, 3, 0>();
      asm volatile("s_nop 0"); SB();
      puts(0); mm<29, 0, 0>();
      puts(1); mm<29, 1, 0>();
      puts(2); mm<29, 2, 0>();
      puts(3);
      CFENCE(); *(volatile LAS unsigned*)(size_t)po = po; CFENCE(); SB();
      mm<29, 3, 0>();
    }
  }
  template <int I> DI void pre() { if constexpr (I < PW) { lda<I, 0>(); lda<I, 1>(); SB(); pre<I + 1>(); } }
  template <int I, bool LD> DI void prew() {
    if constexpr (I < 4 * PRE) {
      if constexpr (LD && I % 3 == 0 && I / 3 < 2 * PW) lda<(I / 6), (I / 3) & 1>();
      ldw<(I >> 2), (I & 3), 0>(); ldw<(I >> 2), (I & 3), 1>();
      SB();
      prew<I + 1, LD>();
    }
  }
};

DI void phase2(const Args& a, const Geo& g, int w TS_ARG) {
  const int lane = fresh_lane();
  unsigned ts = 0;
  static_assert(P2::PW >= 4 && P2::PW <= 5 && P2::PRE == 8, "peek / refill schedule");
  P2 P;
  P.w = w; P.kh = w & 1; P.la = lane * 16;
  P.ba = both_addr(lane);
  const unsigned l0 = peek(C_LVL), w0 = peek2(P.ba, F_W0);
  const u64 tag = get_tag();
  const unsigned gq = lane >> 4, q = (lane >> 2) & 3, p = lane & 3, tok = lane & 15;
  P.rw = __builtin_amdgcn_make_buffer_rsrc(a.ws, 0, WS_END, 0x00020000);
  P.xa = L_FREE + w * 256 + lane * 4;
  UNROLL for (int b = 0; b < 2; ++b) {
    P.va[b] = WS_ACT + ((g.tb64 * 4 + (w ^ b)) * NSTEP + P.kh) * 1024 + lane * 16;
    asm volatile("" : "+v"(P.va[b]));
  }
  const unsigned row = 4 * gq + q, sw = (row >> 1) & 3;
  UNROLL for (int nb = 0; nb < 4; ++nb) {
    P.wa[0][nb] = P.kh * 4096 + row * 128 + ((nb ^ sw) * 32) + p * 8;
    asm volatile("" : "+v"(P.wa[0][nb]));
  }
  const unsigned xoff = (g.tb64 * 64 + w * 16 + tok) * 1024 + g.nt * 128 + (gq & 1) * 32 + (gq >> 1) * 16;
  const int lvl = 3 - 2 * (g.ct >> 3);
  const unsigned fo = WS_FA + (g.rt * 16 + tok) * 256 + lvl * 64 + g.g8 * 8;
  SB();
  u32x4 xr[2];
  if (__builtin_amdgcn_ballot_w64(w0 == 0 || l0 == 0) != 0) {
    const bool late = __builtin_amdgcn_ballot_w64(w0 == 0) != 0;
    if (late) { publish<4>(C_A0 + lvl, (char*)a.ws, fo, tag); spin_lds(P.ba + 8 * F_W0, 1); }
    P.prew<0, false>();
    P.sig(M_PRE);
    if (!late) publish<4>(C_A0 + lvl, (char*)a.ws, fo, tag);
    TS(6);
    UNROLL for (int j = 0; j < 2; ++j) xr[j] = *(const GAS u32x4*)((const char*)a.x + xoff + j * 64);
    lds_wait(C_LVL, 1);
    TS(10);
    P.pre<0>();
  } else {
    CFENCE();
    TS(10);
    P.prew<0, true>();
    P.sig(M_PRE);
    UNROLL for (int j = 0; j < 2; ++j) xr[j] = *(const GAS u32x4*)((const char*)a.x + xoff + j * 64);
    asm volatile("" ::"v"(P.av[0][0]) : "memory");
    publish<4, false>(C_A0 + lvl, (char*)a.ws, fo, tag);
    TS(6);
  }
  TS(9);
  SB();
  P.xr[0] = xr[0]; P.xr[1] = xr[1];
  P.half<0>(ts);
  TS(13);
  mfma_done();
  f32x2 (&r)[8] = P.r;
  UNROLL for (int i = 0; i < 8; ++i) r[i] = fma2((i & 1) ? hi2(P.s[0][i >> 1]) : lo2(P.s[0][i >> 1]), P.sg[i >> 1][i & 1], r[i]);
  UNROLL for (int i = 0; i < 8; ++i) asm volatile("" : "+v"(r[i]));
  f32x4 sx[4];
  {
    unsigned v, n;
    asm volatile("s_mov_b32 %5, %8\n"
                 "1:\n\t"
                 "ds_read_b32 %4, %6\n\t"
                 "ds_read_b128 %0, %7\n\t"
                 "ds_read_b128 %1, %7 offset:1024\n\t"
                 "ds_read_b128 %2, %7 offset:2048\n\t"
                 "ds_read_b128 %3, %7 offset:3072\n\t"
                 "s_sub_u32 %5, %5, 1\n\t"
                 "s_waitcnt lgkmcnt(0)\n\t"
                 "v_cmp_eq_u32 vcc, 0, %4\n\t"
                 "s_cbranch_scc1 2f\n\t"
                 "s_cbranch_vccnz 1b\n"
                 "2:"
                 : "=&v"(sx[0]), "=&v"(sx[1]), "=&v"(sx[2]), "=&v"(sx[3]), "=&v"(v), "=&s"(n)
                 : "v"(P.pf), "v"(P.ps), "i"(SPIN_CAP) : "memory", "vcc", "scc");
  }
  const __amdgpu_buffer_rsrc_t ro = __builtin_amdgcn_make_buffer_rsrc(a.out, 0, SEQ * DM * 2, 0x00020000);
  UNROLL for (int j = 0; j < 2; ++j) {
    unsigned o[4];
    EACH4 o[i] = pk2(fma2(half(sx[2 * j], sx[2 * j + 1], i), P.sg[2 * j + (i >> 1)][i & 1], r[4 * j + i]));
    swap16(o[0], o[2]); swap16(o[1], o[3]);
    __builtin_amdgcn_raw_buffer_store_b128(u32x4{o[0], o[1], o[2], o[3]}, ro, xoff + j * 64, 0, OUT_AUX);
  }
  TS(14);
}

__global__ __launch_bounds__(512, 2) void fused_block(Args) {
  __shared__ __attribute__((aligned(16))) char smem[LDS_BYTES];
  TS_DECL;
  TS(15);
  u64 pc;
  u64x8 ka;
  asm volatile("s_load_dwordx16 %0, %2, 0\n\ts_getpc_b64 %1\n\ts_nop 0" : "=&s"(ka), "=&s"(pc) : "s"(__builtin_amdgcn_kernarg_segment_ptr()));
  const int lane = threadIdx.x & 63;
  int w = __builtin_amdgcn_readfirstlane(threadIdx.x >> 6);
  asm volatile("" ::"v"((unsigned)(size_t)(LAS char*)smem) : "memory");
  const int j = blockIdx.x & 7, i = blockIdx.x >> 3, cl = i >> 3;
  int role = cl & 1;
  touch((const char*)(pc & ~127ull) + min(i * 16 + (int)(threadIdx.x >> 5), CODE_LINES - 1) * 128);
  *(LAS unsigned*)(size_t)(L_FREE + threadIdx.x * 4) = 0;
  SB();
  Geo g;
  g.rt = 8 * (j >> 2) + (i & 7);
  g.g8 = 2 * (j & 3) + (cl >> 1);
  g.u0 = min(16 * role + (2 + 7 * role) * g.g8, 77);
  g.ct = role * 8 + g.g8;
  g.rot = ((i & 7) + 2 * cl + (j & 1)) & (ROT_MODE == 1 ? 0 : ROT_MODE == 2 ? 4 : ROT_MODE == 4 ? 6 : 7);
  g.tb64 = 4 * j + (i & 3);
  g.nt = i >> 2;
  Ld L;
  unsigned k0 = g.rot * 64;
  {
    const unsigned r8 = lane >> 3, c8 = lane & 7, mb = w & 1;
    L.vx0 = r8 * 1024 + (c8 ^ (r8 >> 1)) * 16; L.vx1 = L.vx0 ^ 64;
    L.va0 = __umul24(r8, 3072) + (c8 ^ (r8 & 2)) * 16; L.va1 = L.va0 ^ 64;
    L.xo = (g.rt * 128 + mb * 64) * 1024; L.go = g.u0 * 32;
    L.ao = g.g8 * 128 + 1024 + mb * 1024;
    asm volatile("" : "+v"(L.vx0), "+v"(L.vx1), "+v"(L.va0), "+v"(L.va1));
    asm volatile("" : "+s"(L.xo), "+s"(L.ao), "+s"(L.go), "+s"(k0));
  }
  SB();
  asm volatile("s_waitcnt lgkmcnt(0)" : "+s"(ka) :: "memory");
  Args a;
  a.x = (const void*)ka[0]; a.wqkv = (const void*)ka[1]; a.wo = (const void*)ka[2]; a.wg = (const void*)ka[3];
  a.wu = (const void*)ka[4]; a.wd = (const void*)ka[5]; a.out = (void*)ka[6]; a.ws = (void*)ka[7];
  L.sx = mksrd(a.x, SEQ * DM * 2); L.sa = mksrd(a.wqkv, DM * 3072); L.sg = mksrd((w & 1) ? a.wu : a.wg, DM * 2752);
  L.nz = (const void*)pc;
  if (__builtin_expect(w < 2, 1)) {
    TSD(0);
    dma8<-1>(w * 8192, L.sx, L.xo + k0 * 2, 8192, L.vx0, L.vx1);
  }
  asm volatile("s_barrier" : "+s"(w) :: "memory");
  if (__builtin_expect(w < 2, 1)) {
    if (!role) {
      const unsigned k1 = (k0 + 64) & 511;
      dma8<-1>(L_A + w * 8192, L.sa, L.ao + k0 * 3072, 8 * 3072, L.va0, L.va1);
      landed(F_FULL, w, L.nz);
      dma8<-1>(16384 + w * 8192, L.sx, L.xo + k1 * 2, 8192, L.vx0, L.vx1);
      dma8<-1>(L_A + 16384 + w * 8192, L.sa, L.ao + k1 * 3072, 8 * 3072, L.va0, L.va1);
      landed(F_FULL + 1, w, L.nz);
    }
    TSD(1);
  }
  asm volatile("" : "+s"(w), "+s"(role));
  if (__builtin_expect(w < 2, 1)) {
    __builtin_amdgcn_s_setprio(LOADER_PRIO);
    if (role) loader<1>(a, g, L, w TS_PASS); else loader<0>(a, g, L, w TS_PASS);
  } else if (w < 4) {
    __builtin_amdgcn_s_setprio(3);
    const u64 e = __hip_atomic_load((GAS u64*)((char*)a.ws + WS_EPOCH), __ATOMIC_RELAXED, __HIP_MEMORY_SCOPE_AGENT);
    if (role) sider<1>(a, g, w & 1, e TS_PASS); else sider<0>(a, g, w & 1, e TS_PASS);
  } else {
    const int cw = w - 4;
    __builtin_amdgcn_s_setprio(2);
    if (role) phase1_ffn(a, g, cw TS_PASS); else phase1_kv(a, g, cw TS_PASS);
    phase2(a, g, cw TS_PASS);
  }
}

extern "C" void kernel_launch(void* const* d_in, const int* in_sizes, int n_in, void* d_out, int out_size, void* d_ws,
                              size_t ws_size, hipStream_t stream) {
  Args a;
  a.x = d_in[0]; a.wqkv = d_in[1]; a.wo = d_in[2]; a.wg = d_in[4]; a.wu = d_in[5]; a.wd = d_in[6];
  a.out = d_out; a.ws = d_ws;
  fused_block<<<256, 512, 0, stream>>>(a);
}
```

```cpp
#include <hip/hip_runtime.h>

typedef unsigned long long u64;
typedef __attribute__((ext_vector_type(8))) __bf16 bf16x8;
typedef __attribute__((ext_vector_type(2))) __bf16 bf16x2;
typedef __attribute__((ext_vector_type(2))) float f32x2;
typedef __attribute__((ext_vector_type(4))) float f32x4;
typedef __attribute__((ext_vector_type(2))) unsigned u32x2;
typedef __attribute__((ext_vector_type(4))) unsigned u32x4;
typedef __attribute__((ext_vector_type(4))) int i32x4;
typedef __attribute__((ext_vector_type(8))) u64 u64x8;
typedef __attribute__((ext_vector_type(4))) short s16x4;
#define DI __device__ __forceinline__
#define LAS __attribute__((address_space(3)))
#define GAS __attribute__((address_space(1)))
#define SB() __builtin_amdgcn_sched_barrier(0)
#define CFENCE() asm volatile("" ::: "memory")
#define UNROLL _Pragma("unroll")
#define NOUNROLL _Pragma("unroll 1")

#define ROT_MODE 2
#define OUT_AUX 16
#define GATHER_AT 4
#define POLL_SLEEP 2
#define PAD_Q 1
#define PAD_KV 1
#define PAD_GU 1
#define PAD_W 1
#define PAD_E 12
#define HOLD 2
#define SIDE_D0 0
#define SIDE_D1 0
#define TOUCH_AT 6
#define P2_PW 5
#define PARKW_PRIO 3
#define PREWARM 1
#define GATHER_AT1 4
#define PS_PRE 7
#define PS_PRIO 0
#define PS_PRIO2 1
#define PS_PRIO3 1
#define GO_S 33
#define ATT_X 32
#define PUB_AT 30
#define POLL_GO 1
#define GATHER_PRIO 3
#define P2MAP 0
#define PFD 3
#define PFK 5
#define PARK0 24
#define PARKF 4
#define FIRST_DRAIN 1
#define KV_AT 10
#define LOADER_PRIO 2
#define XP 0
#define CODE_LINES 490

constexpr int SEQ = 2048, DM = 512, DF = 1376;
constexpr int NSTEP = 59;
constexpr unsigned WS_EPOCH = 0;
constexpr unsigned WS_FA = 4096;
constexpr unsigned WS_FK = WS_FA + 256 * 256;
constexpr unsigned WS_FC = WS_FK + 128 * 128;
constexpr unsigned WS_FT = WS_FC + 256 * 128;
constexpr unsigned WS_FQ = WS_FT + 256 * 128;
constexpr unsigned WS_KP = 262144;
constexpr unsigned WS_ZP = WS_KP + 8 * 16 * 4096;
constexpr unsigned WS_ACT = 1048576;
constexpr unsigned WS_CSP = 10u << 20;
constexpr unsigned WS_CQP = WS_CSP + 256 * 1024;
constexpr unsigned WS_STP = WS_CQP + 128 * 128;
constexpr unsigned WS_END = WS_STP + SEQ * 16;
constexpr unsigned WS_DBG = 16u << 20;
constexpr unsigned L_A = 49152, L_G = 49152, L_U = 104448;
constexpr unsigned L_FX = 8 * 4096;
constexpr unsigned L_SX = 12 * 4096;
constexpr unsigned L_KX = 32 * 1024;
constexpr unsigned L_ZX = 144 * 1024;
constexpr unsigned L_KF = 145 * 1024;
constexpr unsigned L_TC = 152 * 1024;
constexpr unsigned L_CS = 154 * 1024;
constexpr unsigned L_FREE = 156 * 1024;
constexpr unsigned L_CTL = L_FREE + 1024;
constexpr unsigned L_LND = L_CTL + 256;
constexpr unsigned L_NM = L_FREE + 2048;
constexpr unsigned L_RS = L_FREE + 2560;
constexpr unsigned L_DUMP = L_FREE + 3072;
enum { C_A0 = 0  , C_K = C_A0 + 4, C_X, C_KF, C_LVL, C_W1, C_RC, C_P0  , C_G0 = C_P0 + 4  , C_S0 = C_G0 + 4  , C_TAG = 32 };
enum { F_FULL = 0  , F_RDY = 12, F_W0, F_WB, F_WC, F_WO };
enum { M_PRE = 17, M_MID };
constexpr unsigned LDS_BYTES = 163840;
constexpr float GELU_C0 = -2.302208198f;
constexpr u64 TAG_STEP = 0x9E3779B97F4A7C15ull;
constexpr unsigned SPIN_CAP = 1u << 20;

struct Args {
  const void* x; const void* wqkv; const void* wo; const void* wg; const void* wu; const void* wd; void* out; void* ws;
};

DI bf16x8 asbf(u32x4 v) { return __builtin_bit_cast(bf16x8, v); }
DI f32x4 mfma(u32x4 a, u32x4 b, f32x4 c) { return __builtin_amdgcn_mfma_f32_16x16x32_bf16(asbf(a), asbf(b), c, 0, 0, 0); }
DI void mfma_acc(f32x4& c, u32x4 a, u32x4 b) { asm volatile("v_mfma_f32_16x16x32_bf16 %0, %1, %2, %0" : "+v"(c) : "v"(a), "v"(b)); }
DI void mfma_new(f32x4& c, u32x4 a, u32x4 b) { asm volatile("v_mfma_f32_16x16x32_bf16 %0, %1, %2, 0" : "=&v"(c) : "v"(a), "v"(b)); }
template <bool FIRST> DI void mfma_k(f32x4& c, u32x4 a, u32x4 b) { if constexpr (FIRST) mfma_new(c, a, b); else mfma_acc(c, a, b); }
DI void mfma_done() { SB(); asm volatile("s_nop 7\n\ts_nop 1" ::: "memory"); SB(); }
DI f32x4 mfma_k16(u32x2 a, u32x2 b, f32x4 c) {
  return __builtin_amdgcn_mfma_f32_16x16x16bf16_1k(__builtin_bit_cast(s16x4, a), __builtin_bit_cast(s16x4, b), c, 0, 0, 0);
}
DI unsigned pk2(f32x2 v) { return __builtin_bit_cast(unsigned, __builtin_convertvector(v, bf16x2)); }
DI unsigned pk2(float a, float b) { return pk2(f32x2{a, b}); }
DI float rbf(float a) { return __uint_as_float(pk2(a, a) << 16); }
DI float bflo(unsigned w) { return __uint_as_float(w << 16); }
DI float bfhi(unsigned w) { return __uint_as_float(w & 0xffff0000u); }
DI f32x2 lo2(f32x4 v) { return f32x2{v[0], v[1]}; }
DI f32x2 hi2(f32x4 v) { return f32x2{v[2], v[3]}; }
DI f32x2 sp(float a) { return f32x2{a, a}; }
DI f32x2 fma2(f32x2 a, f32x2 b, f32x2 c) { return __builtin_elementwise_fma(a, b, c); }
DI float opq(float a) { asm("" : "+v"(a)); return a; }
DI f32x4 add4(f32x4 a, f32x4 b) { return f32x4{opq(a[0] + b[0]), opq(a[1] + b[1]), opq(a[2] + b[2]), opq(a[3] + b[3])}; }
DI u32x4 lds128(unsigned a) { return *(const LAS u32x4*)(size_t)a; }
DI f32x4 lds128f(unsigned a) { return *(const LAS f32x4*)(size_t)a; }
DI u32x2 ldstr(unsigned a) { return __builtin_bit_cast(u32x2, __builtin_amdgcn_ds_read_tr16_b64_v4i16((LAS s16x4*)(size_t)a)); }
DI u32x4 cat(u32x2 l, u32x2 h) { return u32x4{l[0], l[1], h[0], h[1]}; }
DI i32x4 mksrd(const void* p, unsigned bytes) {
  u64 a = (u64)p; return i32x4{(int)(unsigned)a, (int)((unsigned)(a >> 32) & 0xffffu), (int)bytes, 0x00020000};
}
DI void dma(unsigned lds, unsigned voff, i32x4 srd, unsigned soff) {
  asm volatile("s_mov_b32 m0, %0\n\ts_nop 0\n\tbuffer_load_dwordx4 %1, %2, %3 offen lds" ::"s"(lds), "v"(voff), "s"(srd), "s"(soff) : "memory");
}
DI void touch(const void* p) {
  asm volatile("s_mov_b32 m0, %0\n\ts_nop 0\n\tglobal_load_lds_dword %1, off" ::"s"(L_DUMP), "v"(p) : "memory");
}
template <int N> DI void wait_vm() { asm volatile("s_waitcnt vmcnt(%0)" ::"i"(N) : "memory"); }
DI LAS unsigned* ctl(int i) { return (LAS unsigned*)(size_t)(L_CTL + 4 * i); }
DI u64 get_tag() { return *(const LAS u64*)(size_t)(L_CTL + 4 * C_TAG); }
DI unsigned peek(int i) { CFENCE(); unsigned v = __hip_atomic_load(ctl(i), __ATOMIC_RELAXED, __HIP_MEMORY_SCOPE_WORKGROUP); CFENCE(); return v; }
DI void spin_lds(unsigned addr, unsigned want) {
  unsigned v, n;
  asm volatile("s_mov_b32 %1, %4\n"
               "1:\n\t"
               "ds_read_b32 %0, %2\n\t"
               "s_sub_u32 %1, %1, 1\n\t"
               "s_waitcnt lgkmcnt(0)\n\t"
               "v_cmp_gt_u32 vcc, %3, %0\n\t"
               "s_cbranch_scc1 2f\n\t"
               "s_cbranch_vccnz 1b\n"
               "2:"
               : "=&v"(v), "=&s"(n) : "v"(addr), "s"(want), "i"(SPIN_CAP) : "memory", "vcc", "scc");
}
DI void lds_wait(int i, unsigned want) { spin_lds(L_CTL + 4 * i, want); }
DI void wait_free(unsigned want, int lane) { spin_lds(L_FREE + (lane & 3) * 256, want); }
DI void ensure(unsigned v, int i, unsigned want) {
  if (__builtin_expect((unsigned)__builtin_amdgcn_readfirstlane(v) < want, 0)) lds_wait(i, want);
  CFENCE();
}
DI void landed(int i, int mb, const void* nz) {
  asm volatile("s_mov_b32 m0, %0\n\ts_mov_b64 exec, 1\n\tglobal_load_lds_dword %1, %2\n\ts_mov_b64 exec, -1"
               ::"s"(L_LND + 8 * i + 4 * mb), "v"(0u), "s"(nz) : "memory");
}
DI unsigned both_addr(int lane) { return L_LND + (lane & 1) * 4; }
DI unsigned peek2(unsigned ba, int i) { CFENCE(); unsigned v = *(volatile LAS unsigned*)(size_t)(ba + 8 * i); CFENCE(); return v; }
DI void ensure2(unsigned v, unsigned ba, int i) {
  if (__builtin_expect(__builtin_amdgcn_ballot_w64(v == 0) != 0, 0)) spin_lds(ba + 8 * i, 1);
  CFENCE();
}
DI void lds_inc(int i) {
  asm volatile("s_mov_b64 exec, 1\n\tds_add_u32 %0, %1\n\ts_mov_b64 exec, -1" ::"v"(L_CTL + 4 * i), "v"(1u) : "memory");
}
DI void lds_set(int i) { CFENCE(); *(volatile LAS unsigned*)ctl(i) = 1u; CFENCE(); }
template <int N> DI void set_flags(char* ws, unsigned off, u64 tag) {
  asm volatile("s_mov_b64 exec, %3\n\tglobal_store_dwordx2 %0, %1, %2 sc1\n\ts_mov_b64 exec, -1"
               ::"v"(off), "v"(tag), "s"(ws), "s"((1ull << N) - 1) : "memory");
}
template <int N, bool DRAIN = true> DI void publish(int i, char* ws, unsigned off, u64 tag) {
  unsigned old, so;
  if constexpr (DRAIN) wait_vm<0>();
  asm volatile("s_mov_b64 exec, 1\n\t"
               "ds_add_rtn_u32 %0, %2, %3\n\t"
               "s_waitcnt lgkmcnt(0)\n\t"
               "v_readfirstlane_b32 %1, %0\n\t"
               "s_cmp_eq_u32 %1, %7\n\t"
               "s_cselect_b32 exec_lo, 0xffff, 0\n\t"
               "global_store_dwordx2 %4, %5, %6 sc1\n\t"
               "s_mov_b64 exec, -1"
               : "=&v"(old), "=&s"(so) : "v"(L_CTL + 4 * i), "v"(1u), "v"(off), "v"(tag), "s"(ws), "i"(N - 1) : "memory", "scc");
}
template <int N> DI void poll(char* ws, unsigned off, u64 tag) {
  u64 v; unsigned n;
  asm volatile("s_mov_b32 %1, %6\n\t"
               "s_mov_b64 exec, %8\n"
               "1:\n\t"
               "global_atomic_add_x2 %0, %2, %5, %3 sc0 sc1\n\t"
               "s_waitcnt vmcnt(0)\n\t"
               "v_cmp_ne_u64 vcc, %4, %0\n\t"
               "s_cbranch_vccz 2f\n\t"
               "s_sleep %7\n\t"
               "s_sub_u32 %1, %1, 1\n\t"
               "s_cbranch_scc0 1b\n"
               "2:\n\t"
               "s_mov_b64 exec, -1"
               : "=&v"(v), "=&s"(n)
               : "v"(off), "s"(ws), "v"(tag), "v"(0ull), "i"(SPIN_CAP), "i"(POLL_SLEEP), "s"(N == 64 ? ~0ull : (1ull << (N & 63)) - 1)
               : "memory", "vcc", "scc");
}
DI void poll_levels(char* ws, unsigned off, u64 tag) {
  u64 v; unsigned n, l, vl;
  asm volatile("s_mov_b32 %1, %8\n\t"
               "s_mov_b32 exec_hi, 0\n"
               "1:\n\t"
               "global_atomic_add_x2 %0, %4, %7, %5 sc0 sc1\n\t"
               "s_waitcnt vmcnt(0)\n\t"
               "v_cmp_ne_u64 vcc, %6, %0\n\t"
               "s_ff1_i32_b32 %2, vcc_lo\n\t"
               "s_min_u32 %2, %2, 32\n\t"
               "s_lshr_b32 %2, %2, 3\n\t"
               "v_mov_b32 %3, %2\n\t"
               "ds_write_b32 %10, %3\n\t"
               "s_cbranch_vccz 2f\n\t"
               "s_sleep %9\n\t"
               "s_sub_u32 %1, %1, 1\n\t"
               "s_cbranch_scc0 1b\n"
               "2:\n\t"
               "s_mov_b64 exec, -1"
               : "=&v"(v), "=&s"(n), "=&s"(l), "=&v"(vl)
               : "v"(off), "s"(ws), "v"(tag), "v"(0ull), "i"(SPIN_CAP), "i"(POLL_SLEEP), "v"(L_CTL + 4 * C_LVL)
               : "memory", "vcc", "scc");
}
DI void st128(__amdgpu_buffer_rsrc_t rw, unsigned off, u32x4 v) { __builtin_amdgcn_raw_buffer_store_b128(v, rw, off, 0, 16); }
DI void st64(__amdgpu_buffer_rsrc_t rw, unsigned off, u32x2 v) { __builtin_amdgcn_raw_buffer_store_b64(v, rw, off, 0, 16); }

#define TS(i)
#define TSD(i)
#define TS_DECL
#define TS_ARG
#define TS_PASS

DI int fresh_lane() { int l; asm volatile("v_mbcnt_lo_u32_b32 %0, -1, 0\n\tv_mbcnt_hi_u32_b32 %0, -1, %0" : "=v"(l)); return l; }

DI int slowcol(const void* p) { return (3 - (int)((u64)p >> 7)) & 7; }
struct Geo { int rt, ct, g8, u0, rot, tb64, nt, noh; };

#define DMA1(V) "buffer_load_dwordx4 " V ", %2, %0 offen lds\n\ts_add_u32 m0, m0, 0x400\n\t"
#define DMAF(G) ".if " G "\n\tbuffer_load_dword v255, off, %2, 0\n\t.endif\n\t"
#define DMAS(V) DMA1(V) "s_add_u32 %0, %0, %5\n\t"
#define DMA2 DMAS("%3") DMAF("%c6 > 1") DMAS("%4") DMAF("%c6 > 0")
#define DMA_ARGS : "+s"(so) : "s"(lds), "s"(srd), "v"(v0), "v"(v1), "s"(inc), "i"(GAP) : "memory", "scc", "v255"
template <int GAP> DI void dma8(unsigned lds, i32x4 srd, unsigned so, unsigned inc, unsigned v0, unsigned v1) {
  asm volatile("s_mov_b32 m0, %1\n\ts_nop 0\n\t" DMA2 DMA2 DMA2 DMA2 DMA_ARGS);
}
template <int GAP> DI void dma4(unsigned lds, i32x4 srd, unsigned so, unsigned inc, unsigned v0, unsigned v1) {
  asm volatile("s_mov_b32 m0, %1\n\ts_nop 0\n\t" DMA2 DMA2 DMA_ARGS);
}
template <int GAP, int LOFF, int SOFF> DI void dma2(unsigned lb, i32x4 srd, unsigned sb, unsigned v0, unsigned v1) {
  unsigned t;
  asm volatile("s_add_u32 m0, %1, %c7\n\ts_add_u32 %0, %5, %c8\n\t"
               "buffer_load_dwordx4 %3, %2, %0 offen lds\n\t" DMAF("%c6 > 1")
               "buffer_load_dwordx4 %4, %2, %0 offen offset:1024 lds\n\t" DMAF("%c6 > 0")
               : "=&s"(t) : "s"(lb), "s"(srd), "v"(v0), "v"(v1), "s"(sb), "i"(GAP), "i"(LOFF), "i"(SOFF) : "memory", "scc", "v255");
}
#define DMAN(V, G) DMA1(V) ".if " G "\n\tbuffer_load_dword v255, off, %2, 0\n\t.else\n\ts_nop 0\n\t.endif\n\t"
#define DMAN2(A, B) DMAN(A, "%c12 > 0") DMAN(B, "%c12 > 1")
template <int GAP> DI void dma9(unsigned lds, i32x4 srd, unsigned so, const unsigned (&v)[9]) {
  asm volatile("s_mov_b32 m0, %1\n\ts_nop 0\n\t" DMAN2("%3", "%4") DMAN2("%5", "%6") DMAN2("%7", "%8") DMAN2("%9", "%10") DMAN("%11", "%c12 > 0")
               :: "s"(so), "s"(lds), "s"(srd), "v"(v[0]), "v"(v[1]), "v"(v[2]), "v"(v[3]), "v"(v[4]), "v"(v[5]), "v"(v[6]), "v"(v[7]), "v"(v[8]),
                  "i"(GAP) : "memory", "scc", "v255");
}
constexpr int fpair(int g8) { return (14 + 9 * g8 + (g8 & 1)) / 2; }
constexpr int w2row(int j) {
  return 32 * (j < 6 ? j : j < 38 ? fpair(((j - 6) & 15) >> 1) + 2 * (j & 1) + ((j - 6) >> 4) : j < 42 ? 11 + 9 * (j - 38) : 6);
}
template <int J0, int J1, int PAD> DI void w2fill(unsigned lb, unsigned v0, unsigned v1, i32x4 sd, unsigned sb) {
  if constexpr (J0 < J1) {
    if constexpr (PAD >= 4) wait_vm<PAD - 2>();
    dma2<(PAD >= 4 ? 0 : PAD), J0 * 4096, w2row(J0) * 1024>(lb, sd, sb, v0, v1);
    w2fill<J0 + 1, J1, PAD>(lb, v0, v1, sd, sb);
  }
}
DI unsigned w2addr(int lane, int nb) {
  const unsigned row = 4 * (lane >> 4) + ((lane >> 2) & 3);
  return row * 128 + ((nb ^ ((row >> 1) & 3)) * 32) + (lane & 3) * 8;
}
template <int ROLE> constexpr int wotmp(int k) {
  return ROLE ? (k < 4 ? 21 + k : k < 6 ? 32 + k : k == 6 ? 35 : k == 7 ? 29 : k < 12 ? 4 + k : 13 + k) : (k < 8 ? k : 16 + k);
}
template <int ROLE, int K0, int K1, int PAD> DI void wofill(unsigned lb, unsigned v0, unsigned v1, i32x4 so, unsigned sb) {
  if constexpr (K0 < K1) { dma2<PAD, wotmp<ROLE>(K0) * 4096, K0 * 32768>(lb, so, sb, v0, v1); wofill<ROLE, K0 + 1, K1, PAD>(lb, v0, v1, so, sb); }
}
struct WoF {
  u32x2 wl[8][4], wh[8][4];
  unsigned ra[3][4];
  template <int ROLE> DI void prep(int kh) {
    const int lane = fresh_lane();
    UNROLL for (int nb = 0; nb < 4; ++nb) {
      ra[0][nb] = w2addr(lane, nb) + (12 * ROLE + kh) * 4096; ra[1][nb] = ra[0][nb] + 65536;
      ra[2][nb] = ROLE ? ra[0][nb] + (kh ? 16 : 23) * 4096 : 0;
      asm volatile("" : "+v"(ra[0][nb]), "+v"(ra[1][nb]));
      if constexpr (ROLE == 1) asm volatile("" : "+v"(ra[2][nb]));
    }
  }
  template <int ROLE, int I0, int I1> DI void read() {
    UNROLL for (int i = I0; i < I1; ++i) {
      const int r = wotmp<ROLE>(2 * i) - 12 * ROLE;
      UNROLL for (int nb = 0; nb < 4; ++nb) {
        const unsigned ad = wotmp<ROLE>(2 * i + 1) != wotmp<ROLE>(2 * i) + 1 ? ra[2][nb] : ra[r >> 4][nb] + (r & 15) * 4096;
        wl[i][nb] = ldstr(ad); wh[i][nb] = ldstr(ad + 2048);
      }
    }
    CFENCE();
  }
};

struct Ld { i32x4 sx, sa, sg; const void* nz; unsigned vx0, vx1, va0, va1, xo, ao, go; };
template <int ROLE> DI void loader(const Args& a, const Geo& g, const Ld& L, int mb, u32x4 (&park)[14], WoF& W TS_ARG) {
  const int lane = fresh_lane();
  const unsigned lg = mb ? L_U : L_G;
  unsigned vg[9];
  if constexpr (ROLE == 1) {
    UNROLL for (int i = 0; i < 9; ++i) {
      const unsigned idx = 64 * i + lane, rho = __umul24(idx, 3641) >> 16;
      unsigned cc = idx + 2 * (g.g8 & 1) - __umul24(rho, 18); cc = min(cc, cc - 18);
      vg[i] = __umul24(rho ^ ((rho & 8) >> 1), 2752) + cc * 16;
      dma(lg + i * 1024, vg[i], L.sg, L.go + g.rot * (64 * 2752));
      SB();
    }
    dma9<0>(lg + 9216, L.sg, L.go + (g.rot * 64 + 32) * 2752, vg);
    landed(F_FULL, mb, L.nz);
    TSD(1);
  }
  const unsigned r8 = lane >> 3, c8 = lane & 7;
  const unsigned vw = r8 * 1024 + (c8 ^ (r8 & 6)) * 16;
  const unsigned fa = L_FREE + (lane & 3) * 256;
  constexpr int T0 = ROLE ? 1 : 2, PAD = ROLE ? PAD_Q : PAD_KV;
  unsigned slot = T0, k0 = ((g.rot + T0) & 7) * 64, mark = 0;
  NOUNROLL for (int t = T0; t < 8; ++t) {
    if (__builtin_expect(__any((int)mark < t - 2), 0)) spin_lds(fa, t - 2);
    TSD(2 * t);
    if constexpr (ROLE == 0) {
      dma4<PAD>(slot * 16384 + mb * 8192, L.sx, L.xo + k0 * 2, 8192, L.vx0, L.vx1);
      dma4<PAD>(L_A + mb * 8192 + slot * 16384, L.sa, L.ao + k0 * 3072, 8 * 3072, L.va0, L.va1);
      dma4<PAD>(slot * 16384 + mb * 8192 + 4096, L.sx, L.xo + k0 * 2 + 32768, 8192, L.vx0, L.vx1);
      dma4<PAD>(L_A + mb * 8192 + slot * 16384 + 4096, L.sa, L.ao + (k0 + 32) * 3072, 8 * 3072, L.va0, L.va1);
    } else {
      dma4<PAD>(slot * 16384 + mb * 8192, L.sx, L.xo + k0 * 2, 8192, L.vx0, L.vx1);
      dma9<PAD>(lg + slot * 18432, L.sg, L.go + k0 * 2752, vg);
      dma4<PAD>(slot * 16384 + mb * 8192 + 4096, L.sx, L.xo + k0 * 2 + 32768, 8192, L.vx0, L.vx1);
      dma9<PAD>(lg + slot * 18432 + 9216, L.sg, L.go + (k0 + 32) * 2752, vg);
    }
    mark = *(volatile LAS unsigned*)(size_t)fa;
    landed(F_FULL + t, mb, L.nz);
    slot = slot == 2 ? 0 : slot + 1;
    k0 = (k0 + 64) & 511;
  }
  TS(1);
  const i32x4 sd = mksrd(a.wd, DF * DM * 2), so = mksrd(a.wo, DM * DM * 2);
  const unsigned wsb = mb * 16384 + g.nt * 128, lb = mb * 2048, ba = both_addr(lane);
  const __amdgpu_buffer_rsrc_t rd = __builtin_amdgcn_make_buffer_rsrc((void*)a.wd, 0, DF * DM * 2, 0x00020000);
  auto parks = [&]() {
    UNROLL for (int i = 0; i < 14; ++i) {
      park[i] = __builtin_amdgcn_raw_buffer_load_b128(rd, vw, wsb + (w2row(36 + (i >> 1)) + (i & 1) * 8) * 1024, 0);
      CFENCE();
    }
  };
  const unsigned vw1 = vw + 7168;
  W.prep<ROLE>(mb);
#define WD(a_, b_, p_) w2fill<a_, b_, p_>(lb, vw, vw1, sd, wsb)
#define WO(a_, b_) wofill<ROLE, a_, b_, PAD_W>(lb, vw, vw1, so, wsb)
#define RD(i_) W.read<ROLE, i_, i_ + 2>()
  if constexpr (ROLE == 1) {
    __builtin_amdgcn_s_setprio(0);
    parks();
    spin_lds(fa, 6); WD(8, 12, PAD_W); WO(0, 7);
    TS(2);
    spin_lds(fa, 7); WO(7, 16);
    landed(F_WO, mb, L.nz);
    WD(0, 4, PAD_W);
    spin_lds(fa, 8);
    TS(3);
    WD(4, 8, PAD_E);
    landed(F_W0, mb, L.nz);
    TS(10);
    spin_lds(ba + 8 * F_WO, 1);
    RD(0); WD(16, 17, PAD_E); RD(2); WD(17, 18, PAD_E); RD(4); WD(18, 19, PAD_E); RD(6);
    lds_inc(C_RC);
    WD(19, 21, PAD_E);
    lds_wait(C_RC, 4);
    TS(5);
    WD(12, 16, PAD_E); WD(21, 24, PAD_E);
    landed(F_WB, mb, L.nz);
    WD(24, 36, PAD_E);
  } else {
    const unsigned vh = __umul24(lane >> 2, 2752) + ((lane & 3) ^ ((lane >> 4) & 2)) * 16;
    auto g2 = [&](int t) {
      const unsigned b = (t < 3 ? 6 + t : (t & 3) == 3 ? 5 : t == 6 ? 2 : t - 1) * 16384 + mb * 8192, k = (g.rot * 64 + (t & 3) * 128) & 511;
      if (t >= 4) dma8<PAD_GU>(b, L.sa, g.g8 * 128 + (k + 64 * mb) * 3072, 8 * 3072, L.va0, L.va1);
      else if (!g.noh) dma8<PAD_GU>(b, L.sg, L.go + k * 2752, 16 * 2752, vh, vh);
    };
    const unsigned l2 = min(lane, 58), b2 = 2 * (g.g8 * 16 + g.rt) + mb, i5 = 2 * ((g.g8 & 1) * 8 + (g.rt & 7)) + mb;
    g2(0);
    landed(F_FULL + 8, mb, L.nz);
    g2(1); g2(2);
    landed(F_FULL + 9, mb, L.nz);
    if (!(XP & 4)) touch(l2 < 43 ? (const char*)a.wd + (b2 * 43 + l2) * 128 : (const char*)a.wo + (b2 * 16 + l2 - 43) * 128);
    spin_lds(fa, 6); g2(3);
    spin_lds(fa, 7); g2(4);
    spin_lds(fa, 8); g2(5);
    landed(F_FULL + 10, mb, L.nz);
    TS(6);
    if (!(XP & 8)) touch((l2 < 43 ? (const char*)a.wd + (i5 * 43 + l2) * 1024 : (const char*)a.wo + (i5 * 16 + l2 - 43) * 1024) + slowcol(l2 < 43 ? a.wd : a.wo) * 128);
    WO(0, 8);
    spin_lds(fa, 10); WO(8, 16);
    landed(F_WO, mb, L.nz);
    TS(2);
    lds_wait(C_K, 2); g2(6);
    spin_lds(fa, 12); g2(7);
    landed(F_FULL + 11, mb, L.nz);
    TS(7);
    spin_lds(ba + 8 * F_WO, 1);
    RD(0); WD(32, 33, PAD_W); RD(2); WD(33, 34, PAD_W); RD(4); WD(34, 35, PAD_W); RD(6);
    lds_inc(C_RC);
    __builtin_amdgcn_s_setprio(0);
    WD(35, 36, PAD_W);
    lds_wait(C_RC, 4);
    TS(5);
    WD(0, 8, PAD_W);
    spin_lds(fa, 14); WD(12, 20, PAD_W);
    spin_lds(fa, 15); WD(8, 12, PAD_W);
    landed(F_W0, mb, L.nz);
    TS(10);
    spin_lds(fa, 16);
    TS(3);
    WD(20, 24, PAD_E);
    landed(F_WB, mb, L.nz);
    WD(24, 32, PAD_E);
  }
  landed(F_WC, mb, L.nz);
  if constexpr (ROLE == 0) parks();
  wait_vm<0>();
  TS(4);
}

template <int CTRL> DI float dadd(float v) {
  return opq(v + __uint_as_float(__builtin_amdgcn_update_dpp(0u, __float_as_uint(v), CTRL, 0xf, 0xf, true)));
}
template <int ROLE> DI void sider(const Args& a, const Geo& g, int mb, u64 epoch, WoF& W TS_ARG) {
  const int lane = fresh_lane();
  const unsigned i16 = lane & 15, gq = lane >> 4;
  char* ws = (char*)a.ws;
  const __amdgpu_buffer_rsrc_t rw = __builtin_amdgcn_make_buffer_rsrc(a.ws, 0, WS_END, 0x00020000);
  const f32x4 zero = {0.f, 0.f, 0.f, 0.f};
  const unsigned pe = g.ct * 16 + g.rt;
  auto pick = [&](int s) {
    u32x4 r;
    UNROLL for (int d = 0; d < 4; ++d) r[d] = i16 == 8 * s + 2 * d ? 0x3f80u : (i16 == 8 * s + 2 * d + 1 ? 0x3f800000u : 0u);
    return r;
  };
  u64 tag;
  if (PREWARM && mb == 1) {
    const unsigned gr = (g.g8 * 16 + g.rt) * 128, k = i16 >> 1;
    const unsigned o = k < 2 ? WS_FA + (g.tb64 * 8 + g.nt) * 256 + k * 128 : k == 2 ? WS_FC + pe * 128 : k == 3 ? WS_FT + (g.rt * 16 + g.ct) * 128 : (ROLE ? WS_FQ : WS_FK) + gr;
    asm volatile("s_mov_b64 exec, 0x3ff\n\tglobal_atomic_add %0, %1, %2 sc1\n\ts_mov_b64 exec, -1" ::"v"(o + (lane & 1) * 64), "v"(0u), "s"(ws) : "memory");
  }
  if ((ROLE ? SIDE_D1 : SIDE_D0) != 0) __builtin_amdgcn_s_sleep(ROLE ? SIDE_D1 : SIDE_D0);
  if (mb == 0) {
    u32x4 wv[16];
    const unsigned row = 32 * g.rt + gq;
    const char *p0, *p1;
    unsigned st1 = 4 * 2752;
    if constexpr (ROLE == 0) {
      p0 = (const char*)a.wqkv + row * 3072 + 1024 + (i16 >> 3) * 1024 + g.g8 * 128 + (i16 & 7) * 16;
      p1 = i16 < 8 ? p0 - 1024 : (const char*)(i16 < 12 ? a.wg : a.wu) + row * 2752 + g.u0 * 32 + (i16 & 3) * 16;
      st1 = i16 < 8 ? 4 * 3072 : 4 * 2752;
    } else {
      p0 = (const char*)a.wg + row * 2752 + (g.u0 + (g.g8 & 1)) * 32 + i16 * 16;
      p1 = (const char*)a.wu + row * 2752 + (g.u0 + (g.g8 & 1)) * 32 + i16 * 16;
    }
    TS(0);
    UNROLL for (int i = 0; i < 8; ++i) {
      if (XP & 1) { wv[i] = wv[8 + i] = u32x4{i16, gq, row, 1}; continue; }
      wv[i] = *(const GAS u32x4*)(p0 + i * 4 * (ROLE ? 2752 : 3072));
      wv[8 + i] = *(const GAS u32x4*)(p1 + i * st1);
    }
    SB();
    asm volatile("" : "+v"(epoch) :: "memory");
    tag = epoch + TAG_STEP;
    *(LAS u64*)(size_t)(L_CTL + 4 * C_TAG) = tag;
    SB();
    const u32x4 s0 = pick(0), s1 = pick(1);
    f32x4 c = zero;
    UNROLL for (int i = 0; i < 8; ++i) { c = mfma(s0, wv[i], c); c = mfma(s1, wv[8 + i], c); }
    st128(rw, WS_CSP + pe * 1024 + lane * 16, __builtin_bit_cast(u32x4, c));
    wait_vm<0>();
    set_flags<16>(ws, WS_FC + (g.ct * 16 + i16) * 128 + g.rt * 8, tag);
    TS(1);
    wait_free(ROLE ? GATHER_AT1 : GATHER_AT, lane);
    if (!(XP & 2)) poll<16>(ws, WS_FC + pe * 128 + i16 * 8, tag);
    if (!(XP & 2)) UNROLL for (int i = 0; i < 16; ++i) wv[i] = __builtin_amdgcn_raw_buffer_load_b128(rw, lane * 16, WS_CSP + (g.ct * 16 + i) * 1024, 16);
    SB();
    c = __builtin_bit_cast(f32x4, wv[0]);
    UNROLL for (int i = 1; i < 16; ++i) c = add4(c, __builtin_bit_cast(f32x4, wv[i]));
    asm volatile("" : "+v"(c));
    TS(7);
    wait_free(6, lane);
    *(LAS f32x4*)(size_t)(L_CS + (gq >> 1) * 512 + i16 * 32 + (gq & 1) * 16) = c;
  } else {
    u32x4 xv[8], qv[2];
    const unsigned tok = g.rt * 128 + g.ct * 8 + (lane >> 3);
    const char* px = (const char*)a.x + tok * 1024 + (lane & 7) * 16;
    const char* pq = (const char*)((lane & 2) ? a.wu : a.wg) + (32 * g.rt + (lane >> 2)) * 2752 + (g.u0 + ((g.g8 & 1) ? 0 : 8)) * 32 + (lane & 1) * 16;
    TS(0);
    UNROLL for (int i = 0; i < 8; ++i) xv[i] = (XP & 1) ? u32x4{i16, gq, tok, 1} : *(const GAS u32x4*)(px + i * 128);
    if constexpr (ROLE == 1) { UNROLL for (int i = 0; i < 2; ++i) qv[i] = (XP & 1) ? u32x4{i16, gq, tok, 1} : *(const GAS u32x4*)(pq + i * 16 * 2752); }
    SB();
    asm volatile("" : "+v"(epoch) :: "memory");
    tag = epoch + TAG_STEP;
    const s16x4 ones = {0x3f80, 0x3f80, 0x3f80, 0x3f80};
    f32x4 sm = zero, sq = zero;
    UNROLL for (int i = 0; i < 16; ++i) {
      const s16x4 v = __builtin_bit_cast(s16x4, u32x2{xv[i >> 1][2 * (i & 1)], xv[i >> 1][2 * (i & 1) + 1]});
      sm = __builtin_amdgcn_mfma_f32_4x4x4bf16_1k(ones, v, sm, 0, 0, 0);
      sq = __builtin_amdgcn_mfma_f32_4x4x4bf16_1k(v, v, sq, 0, 0, 0);
    }
    unsigned d = 0;
    UNROLL for (int i = 0; i < 4; ++i) d |= __float_as_uint(sq[i]) & ((lane & 3) == i ? ~0u : 0u);
    const float mean = dadd<0x141>(dadd<0x4E>(dadd<0xB1>(sm[0]))) * (1.f / DM);
    const float var = __builtin_fmaxf(opq(dadd<0x141>(dadd<0x4E>(dadd<0xB1>(__uint_as_float(d)))) * (1.f / DM)) - mean * mean, 0.f);
    const float den = rbf(__builtin_amdgcn_sqrtf(rbf(rbf(var) + 1e-5f)));
    const float rs = __builtin_amdgcn_rcpf(den);
    const f32x4 tc = {-rbf(mean), opq(GELU_C0 * rs), opq(-0.1029432397f * rs * rs * rs), opq(den * den)};
    st128(rw, (lane & 7) == 0 ? WS_STP + tok * 16 : WS_END, __builtin_bit_cast(u32x4, tc));
    if constexpr (ROLE == 1) {
      const u32x4 s = pick(0);
      f32x4 c = zero;
      UNROLL for (int i = 0; i < 2; ++i) c = mfma(s, qv[i], c);
      UNROLL for (int i = 0; i < 4; ++i) c[i] = dadd<0x128>(dadd<0x124>(c[i]));
      st128(rw, (lane & 44) == 0 ? WS_CQP + (g.g8 * 16 + g.rt) * 128 + i16 * 32 + gq * 16 : WS_END, __builtin_bit_cast(u32x4, c));
    }
    wait_vm<0>();
    set_flags<ROLE ? 32 : 16>(ws, gq ? WS_FQ + (g.g8 * 16 + i16) * 128 + g.rt * 8 : WS_FT + (g.rt * 16 + i16) * 128 + g.ct * 8, tag);
    TS(1);
    wait_free(ROLE ? GATHER_AT1 : GATHER_AT, lane);
    if (!(XP & 2)) poll<ROLE ? 32 : 16>(ws, (gq ? WS_FQ + (g.g8 * 16 + g.rt) * 128 : WS_FT + (g.rt * 16 + g.ct) * 128) + i16 * 8, tag);
    UNROLL for (int i = 0; i < 2; ++i) qv[i] = __builtin_amdgcn_raw_buffer_load_b128(rw, lane * 16, WS_STP + g.rt * 2048 + i * 1024, 16);
    f32x4 c = zero;
    if constexpr (ROLE == 1 && !(XP & 2)) { UNROLL for (int i = 0; i < 2; ++i) xv[i] = __builtin_amdgcn_raw_buffer_load_b128(rw, lane * 16, WS_CQP + g.g8 * 2048 + i * 1024, 16); }
    if constexpr (ROLE == 1) {
      c = add4(__builtin_bit_cast(f32x4, xv[0]), __builtin_bit_cast(f32x4, xv[1]));
      UNROLL for (int i = 0; i < 4; ++i) {
        unsigned u = __float_as_uint(dadd<0x128>(c[i]));
        { auto r = __builtin_amdgcn_permlane16_swap(u, u, false, false); u = __float_as_uint(opq(__uint_as_float(r[0]) + __uint_as_float(r[1]))); }
        { auto r = __builtin_amdgcn_permlane32_swap(u, u, false, false); c[i] = opq(__uint_as_float(r[0]) + __uint_as_float(r[1])); }
      }
    }
    asm volatile("" : "+v"(c), "+v"(qv[0]), "+v"(qv[1]));
    TS(7);
    wait_free(6, lane);
    UNROLL for (int i = 0; i < 2; ++i) {
      const unsigned t = 64 * i + lane;
      *(LAS u32x4*)(size_t)(L_TC + t * 16) = qv[i];
      if constexpr (ROLE == 0) {
        *(LAS unsigned*)(size_t)(L_NM + t * 4) = qv[i][0];
        *(LAS float*)(size_t)(L_RS + t * 4) = opq(__uint_as_float(qv[i][1]) * (1.f / GELU_C0));
      }
    }
    if constexpr (ROLE == 1) *(LAS f32x4*)(size_t)(L_CS + 1024 + (lane & 7) * 16) = c;
  }
  CFENCE(); *(volatile LAS unsigned*)(size_t)(L_LND + 8 * F_RDY + 4 * mb) = 1u; CFENCE();
  TS(2);
  if (ROLE == 1 && mb == 1 && blockIdx.x == 64) {
    NOUNROLL for (int k = 0; k < 4; ++k) {
      poll<64>(ws, WS_FT + (k * 4 + gq) * 2048 + i16 * 8, tag);
      poll<64>(ws, WS_FC + (k * 4 + gq) * 2048 + i16 * 8, tag);
    }
    set_flags<1>(ws, WS_EPOCH, tag);
  }
  auto woread = [&]() {
    W.prep<ROLE>(mb);
    wait_free(ROLE ? 8 : 12, lane);
    spin_lds(both_addr(lane) + 8 * F_WO, 1);
    W.read<ROLE, 0, 8>();
    lds_inc(C_RC);
    TS(9);
  };
  if constexpr (ROLE == 0) {
    const unsigned kb = g.g8 * 16 + g.rt;
    lds_wait(C_X, 4);
    TS(3);
    UNROLL for (int j = 0; j < 2; ++j) {
      const unsigned o = (2 * mb + j) * 1024 + lane * 16;
      const f32x4 v = add4(add4(lds128f(L_KX + o), lds128f(L_KX + 4096 + o)), add4(lds128f(L_KX + 8192 + o), lds128f(L_KX + 12288 + o)));
      st64(rw, WS_KP + kb * 2048 + (2 * mb + j) * 512 + (gq >> 1) * 256 + i16 * 16 + (gq & 1) * 8, u32x2{pk2(lo2(v)), pk2(hi2(v))});
    }
    const unsigned zo = mb * 128 + (lane & 7) * 16;
    st128(rw, lane < 8 ? WS_ZP + kb * 256 + zo : WS_END,
          __builtin_bit_cast(u32x4, add4(add4(lds128f(L_ZX + zo), lds128f(L_ZX + 256 + zo)), add4(lds128f(L_ZX + 512 + zo), lds128f(L_ZX + 768 + zo)))));
    publish<2>(C_K, ws, WS_FK + (g.g8 * 16 + i16) * 128 + g.rt * 8, tag);
    TS(5);
    woread();
    poll<16>(ws, WS_FK + kb * 128 + i16 * 8, tag);
    TS(4);
    __builtin_amdgcn_s_setprio(GATHER_PRIO);
    u32x4 pv[16], zv[2];
    UNROLL for (int j = 0; j < 2; ++j)
      zv[j] = __builtin_amdgcn_raw_buffer_load_b128(rw, i16 * 256 + gq * 16, WS_ZP + g.g8 * 4096 + (2 * mb + j) * 64, 16);
    UNROLL for (int i = 0; i < 16; ++i)
      pv[i] = __builtin_amdgcn_raw_buffer_load_b128(rw, gq * 2048 + i16 * 16 + (i & 3) * 256, WS_KP + (g.g8 * 16 + (i & 12)) * 2048 + mb * 1024, 16);
    SB();
    const u32x4 s0 = pick(0), s1 = pick(1);
    UNROLL for (int j = 0; j < 2; ++j) {
      f32x4 z = __builtin_bit_cast(f32x4, zv[j]);
      UNROLL for (int i = 0; i < 4; ++i) z[i] = dadd<0x140>(dadd<0x141>(dadd<0x4E>(dadd<0xB1>(z[i]))));
      *(LAS u32x2*)(size_t)(L_KF + 2048 + (2 * mb + j) * 32 + gq * 8) = u32x2{pk2(lo2(z)), pk2(hi2(z))};
    }
    SB();
    f32x4 sum[2] = {zero, zero};
    UNROLL for (int i = 0; i < 16; ++i) sum[(i >> 1) & 1] = mfma((i & 1) ? s1 : s0, pv[i], sum[(i >> 1) & 1]);
    UNROLL for (int j = 0; j < 2; ++j) *(LAS u32x2*)(size_t)(L_KF + (2 * mb + j) * 512 + lane * 8) = u32x2{pk2(lo2(sum[j])), pk2(hi2(sum[j]))};
    __builtin_amdgcn_s_setprio(3);
    lds_inc(C_KF);
    TS(8);
  }
  if constexpr (ROLE == 1) woread();
  if (mb == 0) {
    if constexpr (ROLE == 1) lds_wait(C_A0 + 1, POLL_GO);
    poll_levels(ws, WS_FA + (g.tb64 * 8 + g.nt) * 256 + (lane & 31) * 8, tag);
    TS(6);
  }
}

#define ENSURE_FULL(c, d) ensure2(pk_, ba, c)
struct PF {
  static constexpr int NF = 10, D = PFD, NG = 16 * NF;
  f32x4 acc[NF][4];
  u32x4 xf[2][4];
  u32x2 wl[D + 1], wh[D + 1];
  unsigned xa[2][2], ga[2][2], gs[2][2];
  unsigned pk_, fa, ba;
  f32x4 ec[10];
  unsigned ea[2], eb, ed;
  template <int I> DI void lde() {
    if constexpr (I < 4) ec[4 + I] = lds128f(eb + (I & 1) * 64 + (I >> 1) * 512);
    else if constexpr (I < 6) ec[4 + I] = lds128f(ed + (I & 1) * 64);
    else if constexpr (I >= 2 * D) ec[I - 2 * D] = lds128f(ea[(I - 2 * D) >> 1] + (I & 1) * 256);
  }
  template <int G, int T> DI void ldw() {
    constexpr int kk = G / NF, f = G % NF, c = f % 5 - 1;
    if constexpr (G < NG) {
      const unsigned ad = (c < 0 ? gs[f == 5][T] : ga[c >> 1][T] + (f > 5) * 64 + (c & 1) * 32) + ((kk >> 1) % 3) * 18432 + (kk & 1) * 9216;
      if constexpr (T == 0) wl[G % (D + 1)] = ldstr(ad); else wh[G % (D + 1)] = ldstr(ad);
    } else lde<2 * (G - NG) + T>();
  }
  template <int KK, int TB> DI void ldx() {
    if constexpr (KK < 16) xf[KK & 1][TB] = lds128(xa[KK & 1][TB >> 1] + ((KK >> 1) % 3) * 16384 + (TB & 1) * 2048);
    else lde<2 * D + TB>();
  }
  template <int G, int TB> DI void mm() {
    mfma_k<(G < NF)>(acc[G % NF][TB], cat(wl[G % (D + 1)], wh[G % (D + 1)]), xf[(G / NF) & 1][TB]); SB();
  }
  template <int G> DI void step(unsigned& ts) {
    if constexpr (G < NG) {
      constexpr int kk = G / NF, f = G % NF, s = kk >> 1, nxt = s == 7 ? F_RDY : F_FULL + s + 1;
      constexpr int FC = NF - D;
      static_assert(D == 3 || D == 4, "x reads ride in wide fragments");
      if constexpr ((kk & 1) && f == FC) { ENSURE_FULL(nxt, s); SB(); }
      mm<G, 0>();
      ldw<G + D, 0>(); SB();
      mm<G, 1>();
      ldw<G + D, 1>(); SB();
      if constexpr (f % 5 != 0) {
        mm<G, 2>();
        if constexpr (f == FC || f == FC + 1) ldx<kk + 1, 2 * (f - FC)>();
        if constexpr ((kk & 1) && f == 4) pk_ = peek2(ba, nxt);
        SB();
      }
      if constexpr ((kk & 1) && f == FC - 1) {
        CFENCE(); *(volatile LAS unsigned*)(size_t)fa = s + 1; CFENCE(); SB();
      }
      if constexpr (f % 5 != 0) {
        mm<G, 3>();
        if constexpr (f == FC || f == FC + 1) ldx<kk + 1, 2 * (f - FC) + 1>();
        SB();
      }
      step<G + 1>(ts);
    }
  }
  template <int G> DI void pre() { if constexpr (G < D) { ldw<G, 0>(); ldw<G, 1>(); pre<G + 1>(); } }
};

struct PK {
  static constexpr int D = PFK;
  static constexpr int ng(int grp) { return grp ? 64 : 128; }
  f32x4 acc[8][2];
  u32x4 xf[16][2];
  u32x2 wl[D + 1], wh[D + 1];
  unsigned xa[2], pa[4], pb[2];
  unsigned pk_, fa, ba, fo;
  u64 tag;
  char* ws;
  f32x4 ec[6];
  unsigned ea, eb;
  template <int GRP, int I> DI void lde() {
    if constexpr (GRP == 0) {
      if constexpr (I < 4) ec[I] = lds128f(ea + (I & 1) * 64 + (I >> 1) * 512);
      else if constexpr (I < 12) ec[4 + ((I - 4) >> 2)][I & 3] = *(const LAS float*)(size_t)(eb + (I - 4) * 64);
    } else {
      if constexpr (I < 2) ec[I] = lds128f(ea + I * 256);
      else if constexpr (I < 6) ec[I] = lds128f(eb + (I - 2) * 64 + (GRP == 1 ? 256 : 0));
    }
  }
  template <int GRP, int G, int T> DI void ldw() {
    if constexpr (G < ng(GRP)) {
      constexpr int u = G >> 4, e = G & 15;
      unsigned ad;
      if constexpr (GRP == 0) ad = pa[e & 3] + (u % 3) * 16384 + ((e >> 2) & 1) * 8192 + (e >> 3) * 4096 + T * 512;
      else if constexpr (GRP == 1) ad = pb[e & 1] + ((u + 1) & 3) * 16384 + ((e >> 1) & 1) * 8192 + (e >> 2) * 2048 + T * 256;
      else ad = pa[e & 3] + (u == 3 ? 3 : (u + 1) % 3) * 16384 + (e >> 2) * 4096 + T * 512;
      if constexpr (T == 0) wl[G % (D + 1)] = ldstr(ad); else wh[G % (D + 1)] = ldstr(ad);
    } else lde<GRP, 2 * (G - ng(GRP)) + T>();
  }
  template <int KK, int TB> DI void ldx() {
    if constexpr (KK == 16) lde<0, 2 * D + TB>();
    else xf[KK][TB] = lds128(xa[KK & 1] + ((KK >> 1) % 3) * 16384 + TB * 2048);
  }
  template <int GRP, int G, int TB> DI void mm() {
    const u32x4 wf = cat(wl[G % (D + 1)], wh[G % (D + 1)]);
    if constexpr (GRP == 0) mfma_k<(G < 8)>(acc[G & 7][TB], xf[G >> 3][TB], wf);
    else mfma_k<(G < 4)>(acc[G & 3][TB], wf, xf[G >> 2][TB]);
    SB();
  }
  template <int GRP, int G> DI void step(unsigned& ts) {
    if constexpr (G < ng(GRP)) {
      constexpr int e = G & 15, s = (GRP ? 4 + 4 * GRP : 0) + (G >> 4);
      constexpr int nxt = s < 7 ? F_FULL + s + 1 : s == 7 ? F_RDY : s == 8 ? F_FULL + 9 : s == 10 ? F_FULL + 10 : s == 13 ? F_FULL + 11 : -1;
      static_assert(D >= 5 && D <= 6, "constant slots");
      if constexpr (e == 8 && nxt >= 0) { pk_ = peek2(ba, nxt); SB(); }
      if constexpr (e == 16 - D) {
        if constexpr (s < 8 || (s & 1) || s == 14) { CFENCE(); *(volatile LAS unsigned*)(size_t)fa = s + 1; CFENCE(); SB(); }
        if constexpr (nxt >= 0) { ENSURE_FULL(nxt, s); SB(); }
      }
      if constexpr (GRP == 2 && G == PUB_AT) { publish<4>(C_A0, ws, fo, tag); TS(8); SB(); }
      mm<GRP, G, 0>();
      ldw<GRP, G + D, 0>();
      if constexpr (GRP == 0 && ((G & 7) == 8 - D || (G & 7) == 9 - D)) ldx<(G >> 3) + 1, (G & 7) - (8 - D)>();
      SB();
      mm<GRP, G, 1>();
      ldw<GRP, G + D, 1>(); SB();
      step<GRP, G + 1>(ts);
    }
  }
  template <int GRP, int G> DI void pre() { if constexpr (G < D) { ldw<GRP, G, 0>(); ldw<GRP, G, 1>(); pre<GRP, G + 1>(); } }
};

#define EACH4 UNROLL for (int i = 0; i < 4; ++i)
DI f32x2 half(const f32x4& a, const f32x4& b, int i) { return i == 0 ? lo2(a) : i == 1 ? hi2(a) : i == 2 ? lo2(b) : hi2(b); }
DI u32x4 geglu8(const f32x4& g0, const f32x4& g1, const f32x4& u0, const f32x4& u1, const f32x4& cg0, const f32x4& cg1,
                const f32x4& cu0, const f32x4& cu1, const f32x4& t0, const f32x4& t1) {
  f32x2 g[4], u[4], a[4];
  u32x4 r;
  auto T = [&](int i, int k) { return sp(i < 2 ? t0[k] : t1[k]); };
  EACH4 g[i] = fma2(half(cg0, cg1, i), T(i, 0), half(g0, g1, i));
  SB(); EACH4 a[i] = g[i] * g[i];
  SB(); EACH4 a[i] = fma2(a[i], T(i, 2), T(i, 1));
  SB(); EACH4 a[i] = a[i] * g[i];
  SB(); EACH4 u[i] = fma2(half(cu0, cu1, i), T(i, 0), half(u0, u1, i));
  SB(); EACH4 a[i] = f32x2{__builtin_amdgcn_exp2f(a[i][0]), __builtin_amdgcn_exp2f(a[i][1])};
  SB(); EACH4 a[i] = fma2(a[i], T(i, 3), T(i, 3));
  SB(); EACH4 u[i] = u[i] * g[i];
  SB(); EACH4 a[i] = f32x2{__builtin_amdgcn_rcpf(a[i][0]), __builtin_amdgcn_rcpf(a[i][1])};
  SB(); EACH4 u[i] = u[i] * a[i];
  SB(); EACH4 r[i] = pk2(u[i]);
  SB();
  return r;
}
DI u32x4 phi8(const f32x2 (&c)[4], const f32x2 (&x)[4], const f32x2 (&y)[4], const f32x2 (&rs)[4]) {
  f32x2 b[4], e[4];
  u32x4 r;
  EACH4 b[i] = fma2(x[i], y[i], c[i]);
  SB(); EACH4 b[i] = fma2(b[i], rs[i], sp(1e-6f));
  SB(); EACH4 e[i] = b[i] * sp(1.44269504f);
  SB(); EACH4 b[i] = f32x2{__builtin_fmaxf(b[i][0], 1e-6f), __builtin_fmaxf(b[i][1], 1e-6f)};
  SB(); EACH4 e[i] = f32x2{__builtin_amdgcn_fmed3f(__builtin_amdgcn_exp2f(e[i][0]), 0.f, 1.f), __builtin_amdgcn_fmed3f(__builtin_amdgcn_exp2f(e[i][1]), 0.f, 1.f)};
  SB(); EACH4 b[i] = b[i] + e[i];
  SB(); EACH4 r[i] = pk2(b[i]);
  SB();
  return r;
}

DI void phase1_kv(const Args& a, const Geo& g, int w TS_ARG) {
  PK P;
  unsigned ts = 0;
  {
    const int lane = fresh_lane();
    const unsigned i16 = lane & 15, gq = lane >> 4, q = (lane >> 2) & 3, p = lane & 3;
    P.fa = L_FREE + w * 256 + lane * 4; P.ba = both_addr(lane);
    asm volatile("" : "+v"(P.ba));
    UNROLL for (int ks = 0; ks < 2; ++ks) {
      P.xa[ks] = (32 * w + i16) * 128 + (((4 * ks + gq) ^ (i16 >> 1)) * 16);
      asm volatile("" : "+v"(P.xa[ks]));
    }
    const unsigned sw = (q >> 1) | ((gq & 1) << 1);
    UNROLL for (int j = 0; j < 4; ++j) {
      P.pa[j] = L_A + (8 * gq + q) * 128 + ((j ^ sw) * 32) + p * 8;
      asm volatile("" : "+v"(P.pa[j]));
    }
    P.ea = L_NM + (32 * w + 4 * gq) * 4; P.eb = L_CS + i16 * 4;
    asm volatile("" : "+v"(P.ea), "+v"(P.eb), "+v"(P.fa));
  }
  TS(0);
  spin_lds(P.ba + 8 * F_FULL, 1);
  TS(1);
  P.ldx<0, 0>(); P.ldx<0, 1>();
  P.pre<0, 0>();
  SB();
  P.step<0, 0>(ts);
  mfma_done();
  TS(2);
  const int lane = fresh_lane();
  {
    const u32x4 ones = {0x3f803f80u, 0x3f803f80u, 0x3f803f80u, 0x3f803f80u};
    const f32x4 zero = {0.f, 0.f, 0.f, 0.f};
    f32x2 nm[4], rs[4];
    EACH4 { nm[i] = half(P.ec[0], P.ec[1], i); rs[i] = half(P.ec[2], P.ec[3], i); }
    f32x4 kv, zz;
    UNROLL for (int j = 0; j <= 4; ++j) {
      u32x4 pk, pv;
      if (j < 4) {
        f32x2 v[4], y[4];
        EACH4 v[i] = fma2(nm[i], sp(P.ec[5][j]), half(P.acc[4 + j][0], P.acc[4 + j][1], i));
        SB(); EACH4 v[i] = v[i] * rs[i];
        SB(); EACH4 pv[i] = pk2(v[i]);
        SB();
        EACH4 { v[i] = half(P.acc[j][0], P.acc[j][1], i); y[i] = sp(P.ec[4][j]); }
        pk = phi8(v, nm, y, rs);
      }
      if (j > 0) {
        *(LAS f32x4*)(size_t)(L_KX + w * 4096 + (j - 1) * 1024 + lane * 16) = kv;
        *(LAS f32x4*)(size_t)(L_ZX + w * 256 + (j - 1) * 64 + (lane >> 4) * 16) = zz;
      }
      if (j == 2) P.pk_ = peek2(P.ba, F_FULL + 8);
      if (j < 4) { kv = mfma(pk, pv, zero); zz = mfma(pk, ones, zero); }
      if (j == 3) {
        SB();
        UNROLL for (int k = 0; k < 4; ++k) { P.pa[k] -= 16384; asm volatile("" : "+v"(P.pa[k])); }
        UNROLL for (int k = 0; k < 2; ++k) {
          P.pb[k] = 5 * 16384 + (8 * (lane >> 4) + ((lane >> 2) & 3)) * 64 + ((k ^ ((lane >> 4) & 1)) * 32) + (lane & 3) * 8;
          asm volatile("" : "+v"(P.pb[k]));
        }
        ensure2(P.pk_, P.ba, F_FULL + 8);
        P.pre<1, 0>();
      }
      SB();
    }
    lds_inc(C_X);
  }
  P.ea = L_TC + (32 * w + (lane & 15)) * 16; P.eb = L_CS + 512 + (lane >> 4) * 16;
  unsigned sa = WS_ACT + (g.rt * 8 + 2 * w) * (NSTEP * 1024) + lane * 16;
  P.fo = WS_FA + (g.rt * 16 + (lane & 15)) * 256 + g.g8 * 8;
  P.ws = (char*)a.ws; P.tag = get_tag();
  asm volatile("" : "+v"(P.ea), "+v"(P.eb), "+v"(sa), "+v"(P.fo), "+v"(P.tag));
  const __amdgpu_buffer_rsrc_t rw = __builtin_amdgcn_make_buffer_rsrc(a.ws, 0, WS_END, 0x00020000);
  TS(7);
  SB();
  if (__builtin_expect(!g.noh, 1)) {
    P.step<1, 0>(ts);
    mfma_done();
    const unsigned ho = sa + (g.u0 < 12 ? g.u0 >> 1 : 42) * 1024;
    UNROLL for (int tb = 0; tb < 2; ++tb) {
      if (tb == 1) { P.pre<2, 0>(); SB(); }
      st128(rw, ho + tb * (NSTEP * 1024),
            geglu8(P.acc[0][tb], P.acc[1][tb], P.acc[2][tb], P.acc[3][tb], P.ec[2], P.ec[3], P.ec[4], P.ec[5], P.ec[tb], P.ec[tb]));
      SB();
    }
  } else {
    CFENCE(); *(volatile LAS unsigned*)(size_t)P.fa = 12; CFENCE();
    spin_lds(P.ba + 8 * (F_FULL + 10), 1);
    P.pre<2, 0>();
  }
  TS(4);
  SB();
  P.step<2, 0>(ts);
  P.pk_ = peek(C_KF);
  mfma_done();
  TS(3);
  const int ln = fresh_lane();
  u32x4 pq[4];
  UNROLL for (int k = 0; k < 4; ++k) {
    const int hp = k >> 1, tb = k & 1;
    f32x2 v[4], x[4], y[4], rs[4];
    const float r1 = P.ec[tb][1] * (1.f / GELU_C0);
    EACH4 { v[i] = half(P.acc[2 * hp][tb], P.acc[2 * hp + 1][tb], i); x[i] = half(P.ec[2 + 2 * hp], P.ec[3 + 2 * hp], i); y[i] = sp(P.ec[tb][0]); rs[i] = sp(r1); }
    pq[k] = phi8(v, x, y, rs);
    asm volatile("" : "+v"(pq[k]));
  }
  ensure(P.pk_, C_KF, 2);
  u32x2 kf[4], zf[4];
  UNROLL for (int j = 0; j < 4; ++j) {
    zf[j] = *(const LAS u32x2*)(size_t)(L_KF + 2048 + j * 32 + (ln >> 4) * 8);
    kf[j] = *(const LAS u32x2*)(size_t)(L_KF + j * 512 + ln * 8);
  }
  const f32x4 zero = {0.f, 0.f, 0.f, 0.f};
  f32x4 num[4][2], den[4][2];
  UNROLL for (int k = 0; k < 4; ++k)
    UNROLL for (int j = 0; j < 2; ++j) den[k][j] = mfma_k16(zf[(k & 2) + j], u32x2{pq[k][2 * j], pq[k][2 * j + 1]}, zero);
  SB();
  float rd[4][2];
  UNROLL for (int k = 0; k < 4; ++k)
    UNROLL for (int j = 0; j < 2; ++j) {
      num[k][j] = mfma_k16(kf[(k & 2) + j], u32x2{pq[k][2 * j], pq[k][2 * j + 1]}, zero);
      rd[k][j] = __builtin_amdgcn_rcpf(opq(den[k][j][0] + 1e-6f));
      SB();
    }
  UNROLL for (int k = 0; k < 4; ++k) {
    u32x4 o;
    f32x2 v[4];
    EACH4 v[i] = half(num[k][0], num[k][1], i) * sp(rd[k][i >> 1]);
    SB(); EACH4 o[i] = pk2(v[i]);
    st128(rw, sa + (k & 1) * (NSTEP * 1024) + (43 + 2 * g.g8 + (k >> 1)) * 1024, o);
    SB();
  }
  TS(5);
}

DI void phase1_ffn(const Args& a, const Geo& g, int w TS_ARG) {
  PF P;
  unsigned ts = 0;
  const int m = w >> 1, n = w & 1;
  unsigned sa[2];
  {
    const int lane = fresh_lane();
    const unsigned i16 = lane & 15, gq = lane >> 4, q = (lane >> 2) & 3, p = lane & 3;
    P.fa = L_FREE + w * 256 + lane * 4; P.ba = both_addr(lane);
    asm volatile("" : "+v"(P.ba));
    UNROLL for (int j = 0; j < 2; ++j) {
      UNROLL for (int hf = 0; hf < 2; ++hf) P.xa[j][hf] = (64 * m + 32 * (hf ^ n) + i16) * 128 + (((4 * j + gq) ^ (i16 >> 1)) * 16);
      const unsigned b = ((8 * gq + 4 * j + q) ^ ((gq & 1) << 2)) * 288 + p * 8;
      P.gs[0][j] = L_G + b + 256; P.gs[1][j] = L_U + b + 256;
      P.ga[0][j] = L_G + b + n * 128; P.ga[1][j] = L_U + b + n * 128;
      sa[j] = WS_ACT + ((g.rt * 8 + 4 * m + 2 * (j ^ n)) * NSTEP + 6 + 2 * g.g8 + n) * 1024 + lane * 16;
      P.ea[j] = L_TC + (64 * m + 32 * (j ^ n) + i16) * 16;
      asm volatile("" : "+v"(P.xa[j][0]), "+v"(P.xa[j][1]), "+v"(sa[j]), "+v"(P.ea[j]));
      asm volatile("" : "+v"(P.gs[0][j]), "+v"(P.gs[1][j]), "+v"(P.ga[0][j]), "+v"(P.ga[1][j]));
    }
    P.ed = L_CS + 1024 + gq * 16; P.eb = L_CS + gq * 16 + n * 256;
    asm volatile("" : "+v"(P.eb), "+v"(P.ed), "+v"(P.fa));
  }
  TS(0);
  spin_lds(P.ba + 8 * F_FULL, 1);
  TS(1);
  P.ldx<0, 0>(); P.ldx<0, 1>(); P.ldx<0, 2>(); P.ldx<0, 3>();
  P.pre<0>();
  SB();
  P.step<0>(ts);
  mfma_done();
  TS(3);
  const __amdgpu_buffer_rsrc_t rw = __builtin_amdgcn_make_buffer_rsrc(a.ws, 0, WS_END, 0x00020000);
  auto sto = [&](int tb) { return sa[tb >> 1] + (tb & 1) * (NSTEP * 1024); };
  const f32x4* t = P.ec;
  f32x4 e2[4];
  u64 tag;
  UNROLL for (int tb = 0; tb < 4; ++tb) {
    st128(rw, sto(tb), geglu8(P.acc[1][tb], P.acc[2][tb], P.acc[3][tb], P.acc[4][tb], P.ec[4], P.ec[5], P.ec[6], P.ec[7], t[tb], t[tb]));
    if (tb == 1) {
      UNROLL for (int i = 0; i < 4; ++i) e2[i] = lds128f(P.eb + 128 + (i & 1) * 64 + (i >> 1) * 512);
      tag = get_tag();
    }
    SB();
  }
  TS(4);
  const int lane = fresh_lane();
  UNROLL for (int tb = 0; tb < 4; ++tb) {
    const u32x4 r = geglu8(P.acc[6][tb], P.acc[7][tb], P.acc[8][tb], P.acc[9][tb], e2[0], e2[1], e2[2], e2[3], t[tb], t[tb]);
    if (tb == HOLD - 1) {
      asm volatile("" ::"v"(r));
      wait_vm<HOLD - 1>();
      publish<4, false>(C_A0 + 1, (char*)a.ws, WS_FA + (g.rt * 16 + (lane & 15)) * 256 + 64 + g.g8 * 8, tag);
      TS(7);
    }
    st128(rw, sto(tb) + 16 * 1024, r);
    SB();
  }
  {
    const u32x4 r = geglu8(P.acc[0][0], P.acc[0][1], P.acc[5][0], P.acc[5][1], P.ec[8], P.ec[8], P.ec[9], P.ec[9], t[0], t[1]);
    const unsigned o = WS_ACT + ((g.rt * 8 + 4 * m + 2 * n) * NSTEP + 38 + (g.g8 >> 1)) * 1024 + (g.g8 & 1) * 512 + lane * 8;
    st64(rw, o, u32x2{r[0], r[1]});
    st64(rw, o + NSTEP * 1024, u32x2{r[2], r[3]});
  }
  TS(5);
}

DI void take4(f32x4 (&d)[4], unsigned flag, unsigned src) {
  unsigned v, n;
  asm volatile("s_mov_b32 %5, %8\n"
               "1:\n\t"
               "ds_read_b32 %4, %6\n\t"
               "ds_read_b128 %0, %7\n\t"
               "ds_read_b128 %1, %7 offset:1024\n\t"
               "ds_read_b128 %2, %7 offset:2048\n\t"
               "ds_read_b128 %3, %7 offset:3072\n\t"
               "s_sub_u32 %5, %5, 1\n\t"
               "s_waitcnt lgkmcnt(0)\n\t"
               "v_cmp_eq_u32 vcc, 0, %4\n\t"
               "s_cbranch_scc1 2f\n\t"
               "s_cbranch_vccnz 1b\n"
               "2:"
               : "=&v"(d[0]), "=&v"(d[1]), "=&v"(d[2]), "=&v"(d[3]), "=&v"(v), "=&s"(n)
               : "v"(flag), "v"(src), "i"(SPIN_CAP) : "memory", "vcc", "scc");
}
struct P2 {
  static constexpr int NW = 22, PW = P2_PW, PRE = 6, DH = 3;
  f32x4 f[2][4];
  u32x4 av[2][NW];
  u32x2 wl[NW][4], wh[NW][4];
  unsigned wa[3][4], va[2], pk_, xa, la, ba, pw, po;
  __amdgpu_buffer_rsrc_t rw;
  int w, kh;
  template <int I, int B> DI void lda() {
    if constexpr (I == 19 || I == 20) {
      const unsigned v = va[B] - (la >> 1);
      av[B][I] = cat(__builtin_amdgcn_raw_buffer_load_b64(rw, v, I * 2048, 16), __builtin_amdgcn_raw_buffer_load_b64(rw, v + 512, I * 2048, 16));
    }
    else if constexpr (I == 21)
      av[B][I] = __builtin_amdgcn_raw_buffer_load_b128(rw, va[B] + (kh << 30), I * 2048, 16);
    else if constexpr (I < NW) av[B][I] = __builtin_amdgcn_raw_buffer_load_b128(rw, va[B] + (I & 1) * 2048, (I >> 1) * 4096, 16);
  }
  template <int I, int NB, int T> DI void ldw() {
    if constexpr (I < NW) {
      constexpr int reg = I < 18 ? 2 * I : 2 * I - 36;
      const unsigned ad = wa[reg >> 4][NB] + (reg & 15) * 4096 + T * 2048;
      if constexpr (T == 0) wl[I][NB] = ldstr(ad); else wh[I][NB] = ldstr(ad);
    }
  }
  template <int H, int K> DI void ldh() { if constexpr (H >= 2 * PRE) ldw<(H >> 1), 2 * (H & 1) + (K >> 1), (K & 1)>(); }
  template <int I, int NB, int B> DI void mm() { mfma_k<I == 0>(f[B][NB], cat(wl[I][NB], wh[I][NB]), av[B][I]); SB(); }
  template <int H> DI void need() {
    constexpr int I = H >> 1, NB = 2 * (H & 1);
    if constexpr (H & 1) asm volatile("" ::"v"(cat(wl[I][NB + 1], wh[I][NB + 1])));
    else asm volatile("" ::"v"(cat(wl[I][NB + 1], wh[I][NB + 1])), "v"(av[1][I]));
    SB();
  }
  DI void sig(unsigned v) { CFENCE(); *(volatile LAS unsigned*)(size_t)xa = v; CFENCE(); }
  DI void putf(int nb) { *(LAS f32x4*)(size_t)(pw + nb * 1024) = f[1][nb]; SB(); }
  static constexpr int H_WB = 2 * PRE - DH, H_WC = 24 - DH, H_L3 = 2 * (11 - PW), H_W1 = 36 - DH;
  template <int H> DI void half(unsigned& ts) {
    constexpr int I = H >> 1, NB = 2 * (H & 1);
    if constexpr (H == H_WB) ensure2(pk_, ba, F_WB);
    if constexpr (H == H_WC) ensure2(pk_, ba, F_WC);
    if constexpr (H == H_L3) { ensure(pk_, C_LVL, 3); TSD(30); }
    if constexpr (H == H_W1) { ensure(pk_, C_W1, 2); TS(12); }
    SB();
    if constexpr (H < 2 * NW - 4) {
      need<H>();
      mm<I, NB, 0>();
      lda<I + PW, (H & 1)>();
      if constexpr (H == 2 * NW - 6) {
        pw = L_FX + w * 4096 + la; po = L_CTL + 4 * (C_P0 + w);
        asm volatile("" : "+v"(pw), "+v"(po));
      }
      SB();
      mm<I, NB, 1>();
      ldh<H + DH, 0>(); ldh<H + DH, 1>();
      if constexpr (H < 8) { wa[1 + (H >> 2)][H & 3] = wa[0][H & 3] + (1 + (H >> 2)) * 65536; asm volatile("" : "+v"(wa[1 + (H >> 2)][H & 3])); }
      SB();
      mm<I, NB + 1, 0>();
      ldh<H + DH, 2>();
      if constexpr (H + 2 == H_L3) pk_ = peek(C_LVL);
      if constexpr (H + 2 == H_WB) pk_ = peek2(ba, F_WB);
      if constexpr (H + 2 == H_WC) pk_ = peek2(ba, F_WC);
      if constexpr (H + 2 == H_W1) pk_ = peek(C_W1);
      SB();
      mm<I, NB + 1, 1>();
      ldh<H + DH, 3>(); SB();
      if constexpr (H == GO_S) sig(M_MID);
      SB();
      half<H + 1>(ts);
    } else {
      need<40>();
      mm<20, 0, 1>(); ldh<43, 0>(); ldh<43, 1>(); SB();
      mm<20, 1, 1>(); ldh<43, 2>(); ldh<43, 3>(); SB();
      mm<20, 2, 1>(); mm<20, 3, 1>();
      mm<21, 0, 1>(); mm<21, 1, 1>(); mm<21, 2, 1>(); mm<21, 3, 1>();
      mm<20, 0, 0>(); mm<20, 1, 0>(); mm<20, 2, 0>(); mm<20, 3, 0>();
      asm volatile("s_nop 0"); SB();
      putf(0); mm<21, 0, 0>();
      putf(1); mm<21, 1, 0>();
      putf(2); mm<21, 2, 0>();
      putf(3);
      CFENCE(); *(volatile LAS unsigned*)(size_t)po = po; CFENCE(); SB();
      mm<21, 3, 0>();
    }
  }
  template <int I, int E> DI void pre() { if constexpr (I < E) { lda<I, 0>(); lda<I, 1>(); SB(); pre<I + 1, E>(); } }
  template <int I, bool LD> DI void prew() {
    if constexpr (I < 4 * PRE) {
      if constexpr (LD && I % 2 == 0 && I / 2 < 6) lda<(I / 4), (I / 2) & 1>();
      ldw<(I >> 2), (I & 3), 0>(); ldw<(I >> 2), (I & 3), 1>();
      SB();
      prew<I + 1, LD>();
    }
  }
};

DI void phase2_f(const Args& a, const Geo& g, int w TS_ARG) {
  const int lane = fresh_lane();
  unsigned ts = 0;
  static_assert(P2::PW == 5 && P2::PRE == 6 && GO_S >= 12, "peek / refill schedule");
  P2 P;
  P.w = w; P.kh = w & 1; P.la = lane * 16;
  P.ba = both_addr(lane);
  const unsigned l0 = peek(C_LVL), w0 = peek2(P.ba, F_W0);
  const u64 tag = get_tag();
  P.rw = __builtin_amdgcn_make_buffer_rsrc(a.ws, 0, WS_END, 0x00020000);
  P.xa = L_FREE + w * 256 + lane * 4;
  UNROLL for (int b = 0; b < 2; ++b) {
    P.va[b] = WS_ACT + ((g.tb64 * 4 + (w ^ b)) * NSTEP + P.kh) * 1024 + lane * 16;
    asm volatile("" : "+v"(P.va[b]));
  }
  UNROLL for (int nb = 0; nb < 4; ++nb) {
    P.wa[0][nb] = P.kh * 4096 + w2addr(lane, nb);
    asm volatile("" : "+v"(P.wa[0][nb]));
  }
  const int lvl = 3 - (g.ct >> 3);
  const unsigned fo = WS_FA + (g.rt * 16 + (lane & 15)) * 256 + lvl * 64 + g.g8 * 8;
  SB();
  if (__builtin_amdgcn_ballot_w64(w0 == 0 || l0 == 0) != 0) {
    const bool late = __builtin_amdgcn_ballot_w64(w0 == 0) != 0;
    if (late) { publish<4>(C_A0 + lvl, (char*)a.ws, fo, tag); spin_lds(P.ba + 8 * F_W0, 1); }
    P.prew<0, false>();
    P.sig(M_PRE);
    if (!late) publish<4>(C_A0 + lvl, (char*)a.ws, fo, tag);
    TS(6);
    lds_wait(C_LVL, 1);
    TS(10);
    P.pre<0, 3>();
    lds_wait(C_LVL, 2);
    P.pre<3, P2::PW>();
  } else {
    CFENCE();
    TS(10);
    P.prew<0, true>();
    P.sig(M_PRE);
    const bool l2 = (unsigned)__builtin_amdgcn_readfirstlane(l0) >= 2;
    if (l2) P.pre<3, P2::PW>();
    asm volatile("" ::"v"(P.av[0][0]) : "memory");
    publish<4, false>(C_A0 + lvl, (char*)a.ws, fo, tag);
    TS(6);
    if (!l2) { lds_wait(C_LVL, 2); P.pre<3, P2::PW>(); }
  }
  TS(11);
  TS(9);
  SB();
  P.half<0>(ts);
  TS(13);
  mfma_done();
  f32x4 fx[4];
  take4(fx, P.po ^ 4, P.pw ^ 4096);
  __builtin_amdgcn_s_setprio(0);
  UNROLL for (int nb = 0; nb < 4; ++nb) {
    float v[4], d[4];
    EACH4 v[i] = opq(P.f[0][nb][i] + fx[nb][i]);
    EACH4 d[i] = opq(v[i] * -1.44269504f);
    EACH4 d[i] = opq(__builtin_amdgcn_exp2f(d[i]));
    EACH4 d[i] = opq(d[i] + 1.f);
    EACH4 d[i] = opq(__builtin_amdgcn_rcpf(d[i]));
    *(LAS f32x4*)(size_t)((P.pw ^ 4096) + nb * 1024) = f32x4{opq(v[0] * d[0]), opq(v[1] * d[1]), opq(v[2] * d[2]), opq(v[3] * d[3])};
    SB();
  }
  lds_set(C_G0 + w);
  TS(14);
}

DI void swap16(unsigned& a, unsigned& b) { auto r = __builtin_amdgcn_permlane16_swap(a, b, false, false); a = r[0]; b = r[1]; }
DI void phase2_s(const Args& a, const Geo& g, int w, const WoF& W TS_ARG) {
  const int lane = fresh_lane();
  __builtin_amdgcn_s_setprio(PS_PRIO2);
  const unsigned gq = lane >> 4;
  const unsigned xoff = (g.tb64 * 64 + w * 16 + (lane & 15)) * 1024 + g.nt * 128 + (gq & 1) * 32 + (gq >> 1) * 16;
  u32x4 xr[2];
  UNROLL for (int j = 0; j < 2; ++j) xr[j] = *(const GAS u32x4*)((const char*)a.x + xoff + j * 64);
  const __amdgpu_buffer_rsrc_t rw = __builtin_amdgcn_make_buffer_rsrc(a.ws, 0, WS_END, 0x00020000);
  unsigned pw = L_SX + w * 4096 + lane * 16, po = L_CTL + 4 * (C_S0 + w);
  asm volatile("" : "+v"(pw), "+v"(po));
  lds_wait(C_LVL, 4);
  UNROLL for (int j = 0; j < 2; ++j) {
    unsigned a0 = xr[j][0], a1 = xr[j][1], a2 = xr[j][2], a3 = xr[j][3];
    swap16(a0, a2); swap16(a1, a3);
    xr[j] = u32x4{a0, a1, a2, a3};
    asm volatile("" : "+v"(xr[j]));
  }
  wait_free(M_MID, lane);
  TS(11);
  u32x4 av[16];
  UNROLL for (int i = 0; i < 16; ++i) {
    const unsigned ab = WS_ACT + ((g.tb64 * 4 + (i < 8 ? w ^ 1 : w)) * NSTEP + 43 + (w & 1)) * 1024;
    av[i] = __builtin_amdgcn_raw_buffer_load_b128(rw, lane * 16 + (i & 1) * 2048, ab + ((i & 7) >> 1) * 4096, 16);
    SB();
  }
  __builtin_amdgcn_s_setprio(PS_PRIO3);
  TS(12);
  f32x4 s[2][4];
  f32x2 r[8];
  UNROLL for (int k = 0; k < 64; ++k) {
    const int i = k >> 2, nb = k & 3;
    if (i & 7) mfma_acc(s[i >> 3][nb], cat(W.wl[i & 7][nb], W.wh[i & 7][nb]), av[i]);
    else mfma_new(s[i >> 3][nb], cat(W.wl[i & 7][nb], W.wh[i & 7][nb]), av[i]);
    SB();
    if (k >= 40 && k < 48 && !(k & 1)) *(LAS f32x4*)(size_t)(pw + ((k - 40) >> 1) * 1024) = s[0][(k - 40) >> 1];
    if (k == 47) { CFENCE(); *(volatile LAS unsigned*)(size_t)po = po; CFENCE(); }
    if (k >= 48 && k < 56) { const int e = k - 48; r[e] = f32x2{bflo(xr[e >> 2][e & 3]), bfhi(xr[e >> 2][e & 3])}; asm volatile("" : "+v"(r[e])); }
    SB();
  }
  TS(13);
  f32x4 sx[4], sg[4];
  take4(sx, po ^ 4, pw ^ 4096);
  mfma_done();
  UNROLL for (int nb = 0; nb < 4; ++nb) { sx[nb] += s[1][nb]; asm volatile("" : "+v"(sx[nb])); }
  take4(sg, L_CTL + 4 * (C_G0 + w), L_FX + (w ^ 1) * 4096 + lane * 16);
  const __amdgpu_buffer_rsrc_t ro = __builtin_amdgcn_make_buffer_rsrc(a.out, 0, SEQ * DM * 2, 0x00020000);
  UNROLL for (int j = 0; j < 2; ++j) {
    unsigned o[4];
    EACH4 o[i] = pk2(fma2(half(sx[2 * j], sx[2 * j + 1], i), half(sg[2 * j], sg[2 * j + 1], i), r[4 * j + i]));
    swap16(o[0], o[2]); swap16(o[1], o[3]);
    __builtin_amdgcn_raw_buffer_store_b128(u32x4{o[0], o[1], o[2], o[3]}, ro, xoff + j * 64, 0, OUT_AUX);
  }
  TS(14);
}
DI void unpark(int w, const u32x4 (&park)[14] TS_ARG) {
  const int lane = fresh_lane();
  wait_free(M_PRE, lane);
  UNROLL for (int i = 0; i < 14; ++i) *(LAS u32x4*)(size_t)(w * 2048 + lane * 16 + (i >> 1) * 4096 + (i & 1) * 1024) = park[i];
  lds_inc(C_W1);
  TS(8);
}

__global__ __launch_bounds__(512, 2) void fused_block(Args) {
  __shared__ __attribute__((aligned(16))) char smem[LDS_BYTES];
  TS_DECL;
  TS(15);
  u64 pc;
  u64x8 ka;
  asm volatile("s_load_dwordx16 %0, %2, 0\n\ts_getpc_b64 %1\n\ts_nop 0" : "=&s"(ka), "=&s"(pc) : "s"(__builtin_amdgcn_kernarg_segment_ptr()));
  const int lane = threadIdx.x & 63;
  int w = __builtin_amdgcn_readfirstlane(threadIdx.x >> 6);
  asm volatile("" ::"v"((unsigned)(size_t)(LAS char*)smem) : "memory");
  const int j = blockIdx.x & 7, i = blockIdx.x >> 3, cl = i >> 3;
  int role = cl & 1;
  touch((const char*)(pc & ~127ull) + min(i * 16 + (int)(threadIdx.x >> 5), CODE_LINES - 1) * 128);
  *(LAS unsigned*)(size_t)(L_FREE + threadIdx.x * 4) = 0;
  SB();
  Geo g;
  g.rt = 8 * (j >> 2) + (i & 7);
  g.g8 = 2 * (j & 3) + (cl >> 1);
  g.u0 = 14 + 9 * g.g8;
  g.ct = role * 8 + g.g8;
  g.rot = ((i & 7) + 2 * cl + (j & 1)) & (ROT_MODE == 1 ? 0 : ROT_MODE == 2 ? 4 : ROT_MODE == 4 ? 6 : 7);
  g.tb64 = 4 * j + (i & 3);
  g.nt = i >> 2;
  Ld L;
  unsigned k0 = g.rot * 64;
  {
    const unsigned r8 = lane >> 3, c8 = lane & 7, mb = w & 1;
    L.vx0 = r8 * 1024 + (c8 ^ (r8 >> 1)) * 16; L.vx1 = L.vx0 ^ 64;
    L.va0 = __umul24(r8, 3072) + (c8 ^ (r8 & 2)) * 16; L.va1 = L.va0 ^ 64;
    L.xo = (g.rt * 128 + mb * 64) * 1024; L.go = g.u0 * 32;
    L.ao = g.g8 * 128 + 1024 + mb * 1024;
    asm volatile("" : "+v"(L.vx0), "+v"(L.vx1), "+v"(L.va0), "+v"(L.va1));
    asm volatile("" : "+s"(L.xo), "+s"(L.ao), "+s"(L.go), "+s"(k0));
  }
  SB();
  asm volatile("s_waitcnt lgkmcnt(0)" : "+s"(ka) :: "memory");
  Args a;
  a.x = (const void*)ka[0]; a.wqkv = (const void*)ka[1]; a.wo = (const void*)ka[2]; a.wg = (const void*)ka[3];
  a.wu = (const void*)ka[4]; a.wd = (const void*)ka[5]; a.out = (void*)ka[6]; a.ws = (void*)ka[7];
  L.sx = mksrd(a.x, SEQ * DM * 2); L.sa = mksrd(a.wqkv, DM * 3072); L.sg = mksrd((w & 1) ? a.wu : a.wg, DM * 2752);
  L.nz = (const void*)pc;
  if (__builtin_expect(w < 2, 1)) {
    TSD(0);
    dma8<0>(w * 8192, L.sx, L.xo + k0 * 2, 8192, L.vx0, L.vx1);
  }
  asm volatile("s_barrier" : "+s"(w) :: "memory");
  if (__builtin_expect(w < 2, 1)) {
    if (!role) {
      const unsigned k1 = (k0 + 64) & 511;
      dma8<0>(L_A + w * 8192, L.sa, L.ao + k0 * 3072, 8 * 3072, L.va0, L.va1);
      landed(F_FULL, w, L.nz);
      dma8<0>(16384 + w * 8192, L.sx, L.xo + k1 * 2, 8192, L.vx0, L.vx1);
      dma8<0>(L_A + 16384 + w * 8192, L.sa, L.ao + k1 * 3072, 8 * 3072, L.va0, L.va1);
      landed(F_FULL + 1, w, L.nz);
    }
    TSD(1);
  }
  asm volatile("" : "+s"(w), "+s"(role));
  {
    const int gs = slowcol(a.wqkv);
    g.noh = g.g8 == gs;
    if (!role) { g.u0 = 2 * min(g.g8 - (g.g8 > gs), 6); L.go = g.u0 * 32; }
  }
  if (__builtin_expect(w < 4, 1)) {
    WoF W;
    if (__builtin_expect(w < 2, 1)) {
      u32x4 park[14];
      __builtin_amdgcn_s_setprio(LOADER_PRIO);
      if (role) loader<1>(a, g, L, w, park, W TS_PASS); else loader<0>(a, g, L, w, park, W TS_PASS);
      unpark(w, park TS_PASS);
    } else {
      __builtin_amdgcn_s_setprio(3);
      const u64 e = __hip_atomic_load((GAS u64*)((char*)a.ws + WS_EPOCH), __ATOMIC_RELAXED, __HIP_MEMORY_SCOPE_AGENT);
      if (role) sider<1>(a, g, w & 1, e, W TS_PASS); else sider<0>(a, g, w & 1, e, W TS_PASS);
    }
    phase2_s(a, g, w, W TS_PASS);
  } else {
    const int cw = w - 4;
    __builtin_amdgcn_s_setprio(2);
    if (role) phase1_ffn(a, g, cw TS_PASS); else phase1_kv(a, g, cw TS_PASS);
    phase2_f(a, g, cw TS_PASS);
  }
}

extern "C" void kernel_launch(void* const* d_in, const int* in_sizes, int n_in, void* d_out, int out_size, void* d_ws,
                              size_t ws_size, hipStream_t stream) {
  Args a;
  a.x = d_in[0]; a.wqkv = d_in[1]; a.wo = d_in[2]; a.wg = d_in[4]; a.wu = d_in[5]; a.wd = d_in[6];
  a.out = d_out; a.ws = d_ws;
  fused_block<<<256, 512, 0, stream>>>(a);
}
```
